```python
import torch

def _softmax(x, axis=-1):
    return torch.softmax(x, dim=axis)
def _gelu(x):
    return torch.nn.functional.gelu(x, approximate="tanh")
def _silu(x):
    return torch.nn.functional.silu(x)
def _sigmoid(x):
    return torch.sigmoid(x)
def _layernorm(x, axis=-1, eps=1e-5):
    m = x.mean(dim=axis, keepdim=True)
    v = x.var(dim=axis, keepdim=True, unbiased=False)
    return (x - m) / torch.sqrt(v + eps)
def _scatter_add(tgt, idx, src, axis=0):
    return tgt.clone().index_add_(axis, idx, src)
def _segment_sum(src, seg, n):
    out = torch.zeros((n,) + src.shape[1:], dtype=src.dtype)
    return out.index_add_(0, seg, src)
def _pad(x, pw):
    flat = tuple(v for pair in reversed(pw) for v in pair)
    return torch.nn.functional.pad(x, flat)

kernel_name = 'gated_mix__compressed_kv_cla__sliding_local_dilated__prompt'

def setup_inputs():

    _w = (lambda a: (lambda t: t.to(torch.bfloat16) if t.is_floating_point() else t)(torch.as_tensor(a)))
    def _A_setup():
        B, H, Sq, Sk, D, M = 1, 16, 2048, 2048, 256, 448
        Dc = 4 * D
        return {
            "x": _w((lambda *s: torch.randn(*s, dtype=torch.bfloat16) * 0.05)(B, Sq, M)),
            "c_kv": _w((lambda *s: torch.randn(*s, dtype=torch.bfloat16) * 1.6)(B, Sk, Dc)),
            "w_q": _w((lambda *s: torch.randn(*s, dtype=torch.bfloat16) * 3.2)(M, H, D) * (M ** -0.5)),
            "w_uk": _w((lambda *s: torch.randn(*s, dtype=torch.bfloat16) * 3.2)(Dc, H, D) * (Dc ** -0.5)),
            "w_uv": _w((lambda *s: torch.randn(*s, dtype=torch.bfloat16) * 1.6)(Dc, H, D) * (Dc ** -0.5)),
            "w_o": _w((lambda *s: torch.randn(*s, dtype=torch.bfloat16) * 1.6)(H, D, M) * ((H * D) ** -0.5)),
        }
    _A_in = _A_setup()

    def _B_setup():
        B, S, D = 1, 2048, 448
        return {
            "x": _w((lambda *s: torch.randn(*s, dtype=torch.bfloat16) * 0.05)(B, S, D)),
            "wqkv": _w((lambda *s: torch.randn(*s, dtype=torch.bfloat16) * 1.6)(D, 3 * D)),
            "wo": _w((lambda *s: torch.randn(*s, dtype=torch.bfloat16) * 0.05)(D, D)),
        }
    _B_in = _B_setup()

    return {
        "A_x": _A_in["x"],
        "A_c_kv": _A_in["c_kv"],
        "A_w_q": _A_in["w_q"],
        "A_w_uk": _A_in["w_uk"],
        "A_w_uv": _A_in["w_uv"],
        "A_w_o": _A_in["w_o"],
        "B_x": _B_in["x"],
        "B_wqkv": _B_in["wqkv"],
        "B_wo": _B_in["wo"],
        "gate_w": _w((lambda *s: torch.randn(*s, dtype=torch.bfloat16) * 0.05)(448) * 16.0),
    }

def reference(A_x, A_c_kv, A_w_q, A_w_uk, A_w_uv, A_w_o, B_x, B_wqkv, B_wo, gate_w):
    def _A_ref(x, c_kv, w_q, w_uk, w_uv, w_o):
        B, Sq, M = x.shape[0], x.shape[1], x.shape[2]
        Sk = c_kv.shape[1]
        H, D = w_q.shape[1], w_q.shape[2]
        q = torch.einsum("bqm,mhd->bhqd", x, w_q)
        k = torch.einsum("bkc,chd->bhkd", c_kv, w_uk)
        v = torch.einsum("bkc,chd->bhkd", c_kv, w_uv)
        scores = torch.einsum("bhqd,bhkd->bhqk", q, k) * (D ** -0.5)
        off = Sk - Sq
        qi = torch.arange(Sq)[:, None]
        kj = torch.arange(Sk)[None, :]
        cmask = kj <= (qi + off)
        scores = torch.where(cmask[None, None], scores, -1e9)
        p = _softmax(scores, axis=-1)
        ctx = torch.einsum("bhqk,bhkd->bhqd", p, v)
        return torch.einsum("bhqd,hdm->bqm", ctx, w_o)

    def _B_ref(x, wqkv, wo):
        B, S, D = x.shape
        H = 16; Dh = D // H
        W, dil = 8, 2
        qkv = (lambda x, *d: x.permute(*d))((x @ wqkv).reshape(B, S, 3, H, Dh), 2, 0, 3, 1, 4)
        q, k, v = qkv[0], qkv[1], qkv[2]
        scale = Dh ** -0.5
        pos = torch.arange(S)
        off = torch.arange(W) * dil
        idx = pos[:, None] - off[None, :]
        mask = idx >= 0
        idx_c = torch.where(mask, idx, 0).reshape(-1)
        kg = k[:, :, idx_c, :].reshape(B, H, S, W, Dh)
        vg = v[:, :, idx_c, :].reshape(B, H, S, W, Dh)
        logits = torch.einsum("bhsd,bhswd->bhsw", q, kg) * scale
        logits = torch.where(mask[None, None, :, :], logits, -1e9)
        attn = _softmax(logits, axis=-1)
        o = torch.einsum("bhsw,bhswd->bhsd", attn, vg)
        return (lambda x, *d: x.permute(*d))(o, 0, 2, 1, 3).reshape(B, S, D) @ wo

    x = A_x
    g = _sigmoid((x * gate_w).sum(-1, keepdims=True))
    a = _A_ref(x, A_c_kv, A_w_q, A_w_uk, A_w_uv, A_w_o)
    b = _B_ref(x, B_wqkv, B_wo)
    return g * a + (1 - g) * b
```

```cpp
#include <hip/hip_runtime.h>
#include <hip/hip_bf16.h>
#include <stdint.h>
typedef unsigned short bf16_t;
__device__ __forceinline__ float bf2f(bf16_t x){ return __uint_as_float(((unsigned)x)<<16); }
__device__ __forceinline__ bf16_t f2bf(float f){ unsigned u=__float_as_uint(f); u += 0x7FFF + ((u>>16)&1); return (bf16_t)(u>>16); }

__global__ void gemm_naive(const bf16_t* A, int lda, const bf16_t* B, int ldb, bf16_t* C, int ldc, int M, int N, int K){
  __shared__ float As[16][65]; __shared__ float Bs[16][65];
  int tx=threadIdx.x&15, ty=threadIdx.x>>4;
  int m0=blockIdx.y*64, n0=blockIdx.x*64;
  float acc[4][4]={};
  for(int k0=0;k0<K;k0+=16){
    for(int i=threadIdx.x;i<64*16;i+=256){ int r=i>>4,c=i&15; As[c][r]=bf2f(A[(size_t)(m0+r)*lda+k0+c]); }
    for(int i=threadIdx.x;i<64*16;i+=256){ int r=i>>6,c=i&63; Bs[r][c]=(n0+c<N)?bf2f(B[(size_t)(k0+r)*ldb+n0+c]):0.f; }
    __syncthreads();
    for(int k=0;k<16;++k){
      float a[4],b[4];
      for(int i=0;i<4;++i)a[i]=As[k][ty*4+i];
      for(int j=0;j<4;++j)b[j]=Bs[k][tx*4+j];
      for(int i=0;i<4;++i)for(int j=0;j<4;++j)acc[i][j]+=a[i]*b[j];
    }
    __syncthreads();
  }
  for(int i=0;i<4;++i)for(int j=0;j<4;++j){ int n=n0+tx*4+j; if(n<N) C[(size_t)(m0+ty*4+i)*ldc+n]=f2bf(acc[i][j]); }
}
__global__ void attn_naive(const bf16_t* Q,const bf16_t* Kk,const bf16_t* V,bf16_t* ctx){
  __shared__ float q[256]; __shared__ float s[2048]; __shared__ float red[256];
  int qi=blockIdx.x,h=blockIdx.y,t=threadIdx.x;
  q[t]=bf2f(Q[(size_t)qi*4096+h*256+t]); __syncthreads();
  float mx=-1e30f;
  for(int k=t;k<=qi;k+=256){ const bf16_t* kr=Kk+(size_t)k*4096+h*256; float a=0; for(int d=0;d<256;++d)a+=q[d]*bf2f(kr[d]); a*=0.0625f; s[k]=a; mx=fmaxf(mx,a);}
  red[t]=mx; __syncthreads();
  for(int o=128;o>0;o>>=1){ if(t<o)red[t]=fmaxf(red[t],red[t+o]); __syncthreads(); }
  mx=red[0]; __syncthreads();
  float sum=0; for(int k=t;k<=qi;k+=256){ float e=__expf(s[k]-mx); s[k]=e; sum+=e; }
  red[t]=sum; __syncthreads();
  for(int o=128;o>0;o>>=1){ if(t<o)red[t]+=red[t+o]; __syncthreads(); }
  sum=red[0];
  float a=0; for(int k=0;k<=qi;++k)a+=s[k]*bf2f(V[(size_t)k*4096+h*256+t]);
  ctx[(size_t)qi*4096+h*256+t]=f2bf(a/sum);
}
__global__ void local_naive(const bf16_t* qkv, bf16_t* o){
  int idx=blockIdx.x*blockDim.x+threadIdx.x; if(idx>=2048*16)return;
  int s=idx>>4,h=idx&15;
  float lg[8]; float mx=-1e30f;
  for(int w=0;w<8;++w){ int p=s-2*w; if(p<0){lg[w]=-1e30f;continue;} float a=0; for(int d=0;d<28;++d)a+=bf2f(qkv[(size_t)s*1344+h*28+d])*bf2f(qkv[(size_t)p*1344+448+h*28+d]); lg[w]=a*0.18898223650f; mx=fmaxf(mx,lg[w]); }
  float sum=0; for(int w=0;w<8;++w){ lg[w]=(s-2*w<0)?0.f:__expf(lg[w]-mx); sum+=lg[w]; }
  for(int d=0;d<28;++d){ float a=0; for(int w=0;w<8;++w){ int p=s-2*w; if(p>=0)a+=lg[w]*bf2f(qkv[(size_t)p*1344+896+h*28+d]); } o[(size_t)s*448+h*28+d]=f2bf(a/sum); }
}
__global__ void final_naive(const bf16_t* x,const bf16_t* gw,const bf16_t* a,const bf16_t* b,bf16_t* out){
  int s=blockIdx.x; __shared__ float red[64]; int t=threadIdx.x;
  float acc=0; for(int i=t;i<448;i+=64)acc+=bf2f(x[(size_t)s*448+i])*bf2f(gw[i]);
  red[t]=acc; __syncthreads(); for(int o=32;o>0;o>>=1){ if(t<o)red[t]+=red[t+o]; __syncthreads(); }
  float g=1.f/(1.f+__expf(-red[0]));
  for(int i=t;i<448;i+=64)out[(size_t)s*448+i]=f2bf(g*bf2f(a[(size_t)s*448+i])+(1.f-g)*bf2f(b[(size_t)s*448+i]));
}
extern "C" void kernel_launch(void* const* d_in, const int* in_sizes, int n_in, void* d_out, int out_size, void* d_ws, size_t ws_size, hipStream_t stream){
  const bf16_t* Ax=(const bf16_t*)d_in[0]; const bf16_t* ckv=(const bf16_t*)d_in[1]; const bf16_t* wq=(const bf16_t*)d_in[2];
  const bf16_t* wuk=(const bf16_t*)d_in[3]; const bf16_t* wuv=(const bf16_t*)d_in[4]; const bf16_t* wo=(const bf16_t*)d_in[5];
  const bf16_t* Bx=(const bf16_t*)d_in[6]; const bf16_t* wqkv=(const bf16_t*)d_in[7]; const bf16_t* Bwo=(const bf16_t*)d_in[8]; const bf16_t* gw=(const bf16_t*)d_in[9];
  bf16_t* ws=(bf16_t*)d_ws; bf16_t* Q=ws; bf16_t* K=Q+2048*4096; bf16_t* V=K+2048*4096; bf16_t* ctx=V+2048*4096; bf16_t* a=ctx+2048*4096; bf16_t* qkv=a+2048*448; bf16_t* ob=qkv+2048*1344; bf16_t* b=ob+2048*448;
  gemm_naive<<<dim3(64,32),256,0,stream>>>(Ax,448,wq,4096,Q,4096,2048,4096,448);
  gemm_naive<<<dim3(64,32),256,0,stream>>>(ckv,1024,wuk,4096,K,4096,2048,4096,1024);
  gemm_naive<<<dim3(64,32),256,0,stream>>>(ckv,1024,wuv,4096,V,4096,2048,4096,1024);
  attn_naive<<<dim3(2048,16),256,0,stream>>>(Q,K,V,ctx);
  gemm_naive<<<dim3(7,32),256,0,stream>>>(ctx,4096,wo,448,a,448,2048,448,4096);
  gemm_naive<<<dim3(21,32),256,0,stream>>>(Ax,448,wqkv,1344,qkv,1344,2048,1344,448);
  local_naive<<<128,256,0,stream>>>(qkv,ob);
  gemm_naive<<<dim3(7,32),256,0,stream>>>(ob,448,Bwo,448,b,448,2048,448,448);
  final_naive<<<2048,64,0,stream>>>(Ax,gw,a,b,(bf16_t*)d_out);
}
```

```cpp
#include <hip/hip_runtime.h>
#include <stdint.h>
#include <utility>
#include <type_traits>

typedef unsigned short bf16_t;
typedef __bf16 bf16x8 __attribute__((ext_vector_type(8)));
typedef __bf16 bf16x4 __attribute__((ext_vector_type(4)));
typedef __bf16 bf16x2 __attribute__((ext_vector_type(2)));
typedef float f32x4 __attribute__((ext_vector_type(4)));
typedef float f32x2 __attribute__((ext_vector_type(2)));
typedef unsigned u32x4 __attribute__((ext_vector_type(4)));
typedef __amdgpu_buffer_rsrc_t srd_t;
#define LAS __attribute__((address_space(3)))
#define DI __device__ __forceinline__
#define SB() __builtin_amdgcn_sched_barrier(0)
#define ACL8(b) "a" #b "0","a" #b "1","a" #b "2","a" #b "3","a" #b "4","a" #b "5","a" #b "6","a" #b "7","a" #b "8","a" #b "9"
#define ALL_AGPRS "a0","a1","a2","a3","a4","a5","a6","a7","a8","a9", ACL8(1),ACL8(2),ACL8(3),ACL8(4),ACL8(5),ACL8(6),ACL8(7),ACL8(8),ACL8(9),ACL8(10),ACL8(11),ACL8(12), \
  ACL8(13),ACL8(14),ACL8(15),ACL8(16),ACL8(17),ACL8(18),ACL8(19),ACL8(20),ACL8(21),ACL8(22),ACL8(23),ACL8(24),"a250","a251","a252","a253","a254","a255"

#define KV_DONE_KS 5
#define NA_TILES 18
constexpr int SEQ = 2048, DM = 448, DC = 1024;
constexpr size_t WS_K = 0, WS_V = 16u << 20, WS_Q = 32u << 20, WS_PO = 48u << 20, WS_PART = 64u << 20, WS_LSE = 96u << 20, WS_FLAG = 97u << 20;
constexpr size_t WS_DBG = 100u << 20;
constexpr float LOG2E = 1.4426950408889634f;
constexpr float QSCALE = 0.0625f * LOG2E;
constexpr float BSCALE = 0.1889822365046136f * LOG2E;
constexpr int LDS_BYTES = 163840;

DI unsigned cvtpk(float lo, float hi) { f32x2 v = {lo, hi}; bf16x2 b = __builtin_convertvector(v, bf16x2); return __builtin_bit_cast(unsigned, b); }
DI float dot2bf(unsigned a, unsigned b, float c) { return __builtin_amdgcn_fdot2_f32_bf16(__builtin_bit_cast(bf16x2, a), __builtin_bit_cast(bf16x2, b), c, false); }
DI unsigned rfl(unsigned x) { return __builtin_amdgcn_readfirstlane(x); }
DI srd_t mksrd(const void* p, unsigned bytes) { return __builtin_amdgcn_make_buffer_rsrc((void*)p, (short)0, (int)bytes, 0x00020000); }
template <int... Is, class F> DI void sfor_impl(std::integer_sequence<int, Is...>, F&& f) { (f(std::integral_constant<int, Is>{}), ...); }
template <int N, class F> DI void sfor(F&& f) { sfor_impl(std::make_integer_sequence<int, N>{}, f); }
template <int N> using IC = std::integral_constant<int, N>;

template <int OFF> DI void dma16o(unsigned voff, srd_t srd, unsigned soff) {
  asm volatile("buffer_load_dwordx4 %0, %1, %2 offen offset:%c3 lds" :: "v"(voff), "s"(srd), "s"(soff), "i"(OFF) : "memory");
}
template <int OFF> DI void dma16s(unsigned voff, srd_t srd, unsigned soff) {
  asm volatile("s_nop 4\n\tbuffer_load_dwordx4 %0, %1, %2 offen offset:%c3 lds" :: "v"(voff), "s"(srd), "s"(soff), "i"(OFF) : "memory");
}
template <int OFF> DI void dma16c(unsigned voff, srd_t srd, unsigned soff) {
  asm volatile("buffer_load_dwordx4 %0, %1, %2 offen offset:%c3 sc1 lds" :: "v"(voff), "s"(srd), "s"(soff), "i"(OFF) : "memory");
}
template <int OFF> DI void dma16cs(unsigned voff, srd_t srd, unsigned soff) {
  asm volatile("s_nop 4\n\tbuffer_load_dwordx4 %0, %1, %2 offen offset:%c3 sc1 lds" :: "v"(voff), "s"(srd), "s"(soff), "i"(OFF) : "memory");
}
DI void setm0(unsigned v) { asm volatile("s_mov_b32 m0, %0" :: "s"(v) : "memory"); }
DI void nop1() { asm volatile("s_nop 0"); }
template <int N> DI void waitv() { asm volatile("s_waitcnt vmcnt(%c0)" :: "i"(N) : "memory", ALL_AGPRS); }
template <int N> DI void waitvl() { asm volatile("s_waitcnt vmcnt(%c0) lgkmcnt(0)" :: "i"(N) : "memory", ALL_AGPRS); }
template <int N> DI void waitl() { asm volatile("s_waitcnt lgkmcnt(%c0)" :: "i"(N) : "memory"); }
template <class T> DI void pin(T& a) { asm volatile("" : "+v"(a)); }
template <class T, class... R> DI void pin(T& a, R&... r) { pin(a); pin(r...); }
template <int N> DI void snop() { asm volatile("s_nop %c0" :: "i"(N) : "memory"); }
#define BAR() __builtin_amdgcn_s_barrier()

#define STAMP_DECL
#define STAMP(i)
#define STAMP_D(i)
#define STAMP_OUT(k)
#define ST_ARG
#define ST_PASS

template <int AD> DI void clear16(const u32x4& z) {
  asm volatile("v_mfma_f32_32x32x16_bf16 a[%c1:%c2], %0, %0, 0" :: "v"(z), "i"(AD), "i"(AD + 15) : ALL_AGPRS);
}
DI u32x4 zero4() { unsigned a, b, c, e; asm volatile("s_nop 1\n\tv_mov_b32 %0, 0\n\tv_mov_b32 %1, 0\n\tv_mov_b32 %2, 0\n\tv_mov_b32 %3, 0" : "=v"(a), "=v"(b), "=v"(c), "=v"(e)); u32x4 z = {a, b, c, e}; asm volatile("" : "+v"(z)); return z; }
template <int A> DI void acc_rd4(float (&f)[4]) {
  asm volatile("s_nop 1\n\tv_accvgpr_read_b32 %0, a%c4\n\tv_accvgpr_read_b32 %1, a%c5\n\tv_accvgpr_read_b32 %2, a%c6\n\tv_accvgpr_read_b32 %3, a%c7"
               : "=v"(f[0]), "=v"(f[1]), "=v"(f[2]), "=v"(f[3]) : "i"(A), "i"(A + 1), "i"(A + 2), "i"(A + 3) : ALL_AGPRS);
}
template <int A> DI void acc_rd4n(float (&f)[4]) {
  asm volatile("v_accvgpr_read_b32 %0, a%c4\n\tv_accvgpr_read_b32 %1, a%c5\n\tv_accvgpr_read_b32 %2, a%c6\n\tv_accvgpr_read_b32 %3, a%c7"
               : "=v"(f[0]), "=v"(f[1]), "=v"(f[2]), "=v"(f[3]) : "i"(A), "i"(A + 1), "i"(A + 2), "i"(A + 3) : ALL_AGPRS);
}
template <int A, int B> DI u32x4 acc_pack1n() {
  float f[4], g[4]; acc_rd4n<A>(f); acc_rd4n<B>(g);
  u32x4 v; v[0] = cvtpk(f[0], f[1]); v[1] = cvtpk(f[2], f[3]); v[2] = cvtpk(g[0], g[1]); v[3] = cvtpk(g[2], g[3]);
  return v;
}
template <int A, int B> DI u32x4 acc_packn(float sc) {
  float f[4], g[4]; acc_rd4n<A>(f); acc_rd4n<B>(g);
  u32x4 v; v[0] = cvtpk(f[0] * sc, f[1] * sc); v[1] = cvtpk(f[2] * sc, f[3] * sc); v[2] = cvtpk(g[0] * sc, g[1] * sc); v[3] = cvtpk(g[2] * sc, g[3] * sc);
  return v;
}
template <int A, int B> DI u32x4 acc_pack(float sc) {
  float f[4], g[4]; acc_rd4<A>(f); acc_rd4n<B>(g);
  u32x4 v; v[0] = cvtpk(f[0] * sc, f[1] * sc); v[1] = cvtpk(f[2] * sc, f[3] * sc); v[2] = cvtpk(g[0] * sc, g[1] * sc); v[3] = cvtpk(g[2] * sc, g[3] * sc);
  return v;
}
template <int A, int B> DI u32x4 acc_pack1() {
  float f[4], g[4]; acc_rd4<A>(f); acc_rd4n<B>(g);
  u32x4 v; v[0] = cvtpk(f[0], f[1]); v[1] = cvtpk(f[2], f[3]); v[2] = cvtpk(g[0], g[1]); v[3] = cvtpk(g[2], g[3]);
  return v;
}
template <int AUX> DI void st16(srd_t srd, unsigned voff, unsigned soff, u32x4 v) { __builtin_amdgcn_raw_buffer_store_b128(v, srd, voff, soff, AUX); }

constexpr int XSLOT = 32768, WBASE = 98304, WSLOT = 16384;
struct XStream { srd_t srd; unsigned v[8]; unsigned soff; };
struct WStream { srd_t srd; unsigned v[4]; unsigned soff, gstep; };
DI void issueX(XStream& X, unsigned ldsaddr, int half) {
  setm0(ldsaddr + half * 4096u); nop1();
  dma16o<0>(X.v[4 * half + 0], X.srd, X.soff); dma16o<1024>(X.v[4 * half + 1], X.srd, X.soff);
  dma16o<2048>(X.v[4 * half + 2], X.srd, X.soff); dma16o<3072>(X.v[4 * half + 3], X.srd, X.soff);
  if (half) X.soff += 128u;
}
DI void issueW(WStream& W, unsigned ldsaddr) {
  setm0(ldsaddr); nop1();
  dma16o<0>(W.v[0], W.srd, W.soff); dma16o<1024>(W.v[1], W.srd, W.soff); dma16o<2048>(W.v[2], W.srd, W.soff); dma16o<3072>(W.v[3], W.srd, W.soff);
  W.soff += W.gstep;
}
DI void makeW4(WStream& W, const void* p, unsigned bytes, unsigned ldw, unsigned col0, unsigned w, unsigned lane) {
  W.srd = mksrd(p, bytes);
#pragma unroll
  for (int j = 0; j < 4; ++j) W.v[j] = (2u * j + (lane >> 5)) * ldw + 16u * ((lane & 31u) ^ (((2u * (j & 1) + (lane >> 5)) << 2) | ((w & 1u) << 1))) - 1024u * j;
  W.soff = 8u * w * ldw + col0 * 2u; W.gstep = 32u * ldw;
}
struct Frags { bf16x8 x[8], w[8]; bf16x4 wl[8], wh[8];
  __device__ __forceinline__ void join() {
#pragma unroll
    for (int n = 0; n < 8; ++n) w[n] = __builtin_shufflevector(wl[n], wh[n], 0, 1, 2, 3, 4, 5, 6, 7);
  } };
template <int AD> DI void mfma16_a(const bf16x8& a, const bf16x8& b) {
  asm volatile("v_mfma_f32_16x16x32_bf16 a[%c2:%c3], %0, %1, a[%c2:%c3]" :: "v"(a), "v"(b), "i"(AD), "i"(AD + 3));
}
DI bf16x4 readWh(LAS char* lds, unsigned addr, int off) { return __builtin_amdgcn_ds_read_tr16_b64_v4bf16((LAS bf16x4*)(lds + addr + off)); }
DI bf16x8 readX(LAS char* lds, unsigned addr, int off) { return *(LAS bf16x8*)(lds + addr + off); }

struct Prefetch { srd_t s0; unsigned v0, j0, so, step; };
DI void pf_load(unsigned& j, unsigned voff, srd_t s) { asm volatile("s_nop 4\n\tbuffer_load_dword %0, %1, %2, 0 offen" : "=v"(j) : "v"(voff), "s"(s) : "memory"); }
DI void pf_slot(Prefetch& p) { asm volatile("buffer_load_dword %0, %1, %2, %3 offen" : "+v"(p.j0) : "v"(p.v0), "s"(p.s0), "s"(p.so) : "memory"); p.so += p.step; }
constexpr int kv_hook_ops(int e4) { return e4 == 1 ? 17 : (e4 == 2 ? 5 : (e4 == 3 ? 12 : 0)); }
template <bool VT, class Hook>
DI void kv_loop(Frags& f0, Frags& f1, LAS char* lds, unsigned ldsb, XStream& X, WStream& W, const unsigned (&xb)[2], const unsigned (&wb)[8], unsigned w, Prefetch& pf, Hook hook) {
  constexpr int S = 32;
  unsigned xs = 0;
  auto sub = [&](auto E4, auto LASTT, const Frags& cur, Frags& nxt) {
    constexpr int e4 = decltype(E4)::value, e = e4 & 1;
    constexpr bool LAST = decltype(LASTT)::value;
    constexpr int wo = e4 * WSLOT, won = ((e4 + 1) & 3) * WSLOT;
    unsigned xsn = xs; if (e == 1) xsn = (xs == 2 * XSLOT) ? 0u : xs + XSLOT;
    const unsigned xa = xb[(e + 1) & 1] + xsn;
    const unsigned xl = ldsb + ((e == 1) ? xs : ((xs == 0) ? 2 * XSLOT : xs - XSLOT)) + w * 8192u + ((e + 1) & 1) * 4096u;
    const unsigned wl = ldsb + WBASE + wo + w * 4096u;
    constexpr int hx = 4 * ((e + 1) & 1);
    sfor<64>([&](auto I) {
      constexpr int i = I, m = i / 8, n = i % 8;
      if constexpr (VT) mfma16_a<4 * (8 * m + n)>(cur.x[m], cur.w[n]); else mfma16_a<4 * (8 * m + n)>(cur.w[n], cur.x[m]);
      if constexpr (i == 1) {
        SB();
        if constexpr (!LAST) waitvl<(e == 0 ? 17 : 18)>(); else if constexpr (e4 == 0) waitvl<17>(); else if constexpr (e4 == 1) waitvl<0>(); else if constexpr (e4 == 2) waitvl<kv_hook_ops(1)>(); else waitvl<63>();
        BAR();
      }
      if constexpr (i >= 2 && !(LAST && e4 == 3)) {
        constexpr int k = i - 2;
        if constexpr (k == 0) nxt.wl[0] = readWh(lds, wb[0], won);
        else if constexpr (k == 1) nxt.wh[0] = readWh(lds, wb[0], won + 2048);
        else if constexpr (k == 2) nxt.x[0] = readX(lds, xa, 0);
        else if constexpr (k < 17) {
          constexpr int kk = k - 3, nn = 1 + kk / 2;
          if constexpr ((kk & 1) == 0) nxt.wl[nn] = readWh(lds, wb[nn], won); else nxt.wh[nn] = readWh(lds, wb[nn], won + 2048);
        } else if constexpr (k < 24) { constexpr int mm = 1 + k - 17; nxt.x[mm] = readX(lds, xa, 2048 * mm); }
      }
      if constexpr (!LAST) {
        constexpr int d0 = 26;
        if constexpr (i >= d0 && (i - d0) % 3 == 0 && (i - d0) / 3 < 10) {
          constexpr int d = (i - d0) / 3;
          if constexpr (d == 0) setm0(wl);
          else if constexpr (d == 1) dma16o<0>(W.v[0], W.srd, W.soff);
          else if constexpr (d == 2) dma16o<1024>(W.v[1], W.srd, W.soff);
          else if constexpr (d == 3) dma16o<2048>(W.v[2], W.srd, W.soff);
          else if constexpr (d == 4) dma16o<3072>(W.v[3], W.srd, W.soff);
          else if constexpr (d == 5) setm0(xl);
          else if constexpr (d == 6) dma16o<0>(X.v[hx + 0], X.srd, X.soff);
          else if constexpr (d == 7) dma16o<1024>(X.v[hx + 1], X.srd, X.soff);
          else if constexpr (d == 8) dma16o<2048>(X.v[hx + 2], X.srd, X.soff);
          else dma16o<3072>(X.v[hx + 3], X.srd, X.soff);
        }
        if constexpr (e == 0 && i == 57) pf_slot(pf);
      } else hook(E4, I);
      SB();
    });
    if constexpr (!LAST) { W.soff += W.gstep; if (((e + 1) & 1) == 1) X.soff += 128u; }
    xs = xsn;
    if constexpr (!(LAST && e4 == 3)) nxt.join();
  };
#pragma unroll 1
  for (int s = 0; s < S - 4; s += 4) {
    sub(IC<0>{}, std::false_type{}, f0, f1);
    sub(IC<1>{}, std::false_type{}, f1, f0);
    sub(IC<2>{}, std::false_type{}, f0, f1);
    if (s + 3 == S - 5) X.soff = 0x40000000u;
    sub(IC<3>{}, std::false_type{}, f1, f0);
  }
  sub(IC<0>{}, std::true_type{}, f0, f1);
  sub(IC<1>{}, std::true_type{}, f1, f0);
  sub(IC<2>{}, std::true_type{}, f0, f1);
  sub(IC<3>{}, std::true_type{}, f1, f0);
}

constexpr int AO_(int db, int qb) { return 8 * db + 4 * qb; }
constexpr int AQ_(int qb, int ds) { return 128 + 32 * qb + 4 * ds; }
constexpr int P1_WS = 24576, P1_XB = 98304, P1_XS = 20480, P1_G = 159744;
constexpr int p1_use(int f) { return f < 18 ? 2 * f : (f < 22 ? 36 + 3 * (f - 18) : 48); }
constexpr int P1_LA = 16, P1_R = 14, P1_NF = 14 * 23, P1_BAR = 21;
constexpr int p1_rd(int F) { return 50 * (F / 23) + p1_use(F % 23) - P1_LA; }
constexpr int p1_ni(int F) { return (F % 23) == 22 ? 1 : 2; }
constexpr int p1_reg(int F) { return 200 + 4 * (F % P1_R); }
constexpr int p1_xrd(int ks, int tb) { return 50 * (ks - 1) + 22 + 3 * tb; }
constexpr int p1_lds_between(int g0, bool after_w, int G) {
  int n = 0;
  for (int F = 0; F < P1_NF; ++F) for (int i = 0; i < p1_ni(F); ++i) { const int g = p1_rd(F) + i; if (g > g0 && g < G) ++n; }
  for (int ks = 1; ks < 14; ++ks) for (int tb = 0; tb < 3; ++tb) { const int g = p1_xrd(ks, tb); if ((g > g0 || (g == g0 && after_w)) && g < G) ++n; }
  return n;
}
constexpr bool p1_xiss(int ks) { return (ks & 1) == 1 && (ks + 5) / 2 < 7; }
#define P1_STORE_MODE 0
constexpr int p1_nb(int k) { return P1_STORE_MODE == 0 ? (k < 10 ? 1 : 0) : ((k & 1) == 0 ? (k < 12 ? 2 : 0) : (k < 8 ? 1 : 0)); }
constexpr int p1_na(int k) { return P1_STORE_MODE == 0 ? (k < 8 ? 2 : (k < 10 ? 3 : 0)) : ((k & 1) == 0 ? (k < 8 ? 3 : (k < 12 ? 2 : 0)) : 0); }
constexpr int p1_st0(int k) { int n = 0; for (int j = 0; j < k; ++j) n += p1_nb(j) + p1_na(j); return n; }
constexpr int p1_ops_before_barrier(int ks, int nst) {
  int n = 34 + (nst == 2 ? 32 : 0);
  for (int k = 0; k < ks; ++k) n += (nst == 1 ? 1 : 0) * (p1_nb(k) + p1_na(k)) + (k + 3 < 14 ? 6 : 0) + (p1_xiss(k) ? 5 : 0);
  return n + (nst == 1 ? 1 : 0) * p1_nb(ks);
}
constexpr int p1_wslot(int j) { return ((j + 2) % 4) * P1_WS; }
constexpr int p1_end_W(int j, int nst) {
  if (j == 0) return 11; if (j == 1) return 17; if (j == 2) return 29;
  return p1_ops_before_barrier(j - 3, nst) + 6;
}
constexpr int p1_end_x(int p, int nst) {
  if (p == 0) return 5; if (p == 1) return 22; if (p == 2) return 34;
  const int ks = 2 * p - 5;
  return p1_ops_before_barrier(ks, nst) + (ks + 3 < 14 ? 6 : 0) + 5;
}
constexpr int p1_wait(int ks, int nst) {
  int need = p1_end_W(ks + 1, nst);
  if (ks & 1) { const int e = p1_end_x((ks + 1) / 2, nst); if (e > need) need = e; }
  if (ks == 0 && need < 23) need = 23;
  return p1_ops_before_barrier(ks, nst) - need;
}
constexpr int ABq(int d, int qb) { return 128 + 8 * d + 4 * qb; }
constexpr int ABk(int d, int tb) { return 144 + 12 * d + 4 * tb; }
constexpr int ABv(int d, int tb) { return 168 + 12 * d + 4 * tb; }
constexpr int AZ(int qb) { return 192 + 4 * qb; }

template <int AD, int AF, bool Z> DI void mfK(const u32x4& x) {
  if constexpr (Z) asm volatile("v_mfma_f32_16x16x32_bf16 a[%c1:%c2], a[%c3:%c4], %0, 0" :: "v"(x), "i"(AD), "i"(AD + 3), "i"(AF), "i"(AF + 3));
  else asm volatile("v_mfma_f32_16x16x32_bf16 a[%c1:%c2], a[%c3:%c4], %0, a[%c1:%c2]" :: "v"(x), "i"(AD), "i"(AD + 3), "i"(AF), "i"(AF + 3));
}
template <int AD, int AF, bool Z> DI void mfV(const u32x4& x) {
  if constexpr (Z) asm volatile("v_mfma_f32_16x16x32_bf16 a[%c1:%c2], %0, a[%c3:%c4], 0" :: "v"(x), "i"(AD), "i"(AD + 3), "i"(AF), "i"(AF + 3));
  else asm volatile("v_mfma_f32_16x16x32_bf16 a[%c1:%c2], %0, a[%c3:%c4], a[%c1:%c2]" :: "v"(x), "i"(AD), "i"(AD + 3), "i"(AF), "i"(AF + 3));
}
template <int A, int OFF> DI void tr_rd(unsigned base) { asm volatile("ds_read_b64_tr_b16 a[%c1:%c2], %0 offset:%c3" :: "v"(base), "i"(A), "i"(A + 1), "i"(OFF) : "memory"); }
template <int A, int OFF> DI void frag_rd(unsigned base) { asm volatile("ds_read_b128 a[%c1:%c2], %0 offset:%c3" :: "v"(base), "i"(A), "i"(A + 3), "i"(OFF) : "memory"); }
template <int OFF> DI void lds_rdv(u32x4& r, unsigned base) { asm volatile("ds_read_b128 %0, %1 offset:%c2" : "=v"(r) : "v"(base), "i"(OFF) : "memory"); }
DI void mfma_vv0(f32x4& d, const u32x4& a, const u32x4& b) { asm volatile("v_mfma_f32_16x16x32_bf16 %0, %1, %2, 0" : "=&v"(d) : "v"(a), "v"(b)); }
DI void mfma_vvc(f32x4& d, const u32x4& a, const u32x4& b, const f32x4& c) { asm volatile("v_mfma_f32_16x16x32_bf16 %0, %1, %2, %3" : "=&v"(d) : "v"(a), "v"(b), "v"(c)); }
DI float xmax16_32(float v) {
  auto a = __builtin_amdgcn_permlane32_swap(__float_as_uint(v), __float_as_uint(v), false, false);
  v = fmaxf(__uint_as_float(a[0]), __uint_as_float(a[1]));
  auto b = __builtin_amdgcn_permlane16_swap(__float_as_uint(v), __float_as_uint(v), false, false);
  return fmaxf(__uint_as_float(b[0]), __uint_as_float(b[1]));
}
DI float xsum16_32(float v) {
  auto a = __builtin_amdgcn_permlane32_swap(__float_as_uint(v), __float_as_uint(v), false, false);
  v = __uint_as_float(a[0]) + __uint_as_float(a[1]);
  auto b = __builtin_amdgcn_permlane16_swap(__float_as_uint(v), __float_as_uint(v), false, false);
  return __uint_as_float(b[0]) + __uint_as_float(b[1]);
}
template <int A> DI void acc_wr4(const u32x4& v) {
  asm volatile("v_accvgpr_write_b32 a%c4, %0\n\tv_accvgpr_write_b32 a%c5, %1\n\tv_accvgpr_write_b32 a%c6, %2\n\tv_accvgpr_write_b32 a%c7, %3"
               :: "v"(v[0]), "v"(v[1]), "v"(v[2]), "v"(v[3]), "i"(A), "i"(A + 1), "i"(A + 2), "i"(A + 3) : ALL_AGPRS);
}

struct P1 {
  WStream W; srd_t sB, sX;
  unsigned vB[2], vX[5], sBo, sXo;
};
DI void p1_setup(P1& c, const bf16_t* x, const bf16_t* wq, const bf16_t* wqkv, int head, int T, unsigned w, unsigned lane) {
  makeW4(c.W, wq, DM * 4096 * 2, 8192, head * 256, w, lane);
  c.sB = mksrd(wqkv, DM * 1344 * 2);
#pragma unroll
  for (int jj = 0; jj < 2; ++jj) {
    const unsigned ch = (lane & 15u) ^ ((((lane >> 4) & 3u) << 2) | ((w & 1u) << 1)), sec = ch >> 2;
    c.vB[jj] = (sec == 3u) ? 0x7fffff00u : ((4u * jj + (lane >> 4)) * 2688u + sec * 896u + 56u * head + 16u * (ch & 3u) - 1024u * jj);
  }
  c.sBo = 8u * w * 2688u;
  c.sX = mksrd((const char*)x - 4096, SEQ * DM * 2 + 4096);
#pragma unroll
  for (int j = 0; j < 5; ++j) {
    const int row = 40 * (int)w + 8 * j + (int)(lane >> 3), tok = 128 * T - 16 + row;
    const unsigned sw = (4u * (w + j) + (lane >> 4)) & 7u;
    c.vX[j] = (tok < 0 || tok >= SEQ) ? 0x7fffff00u : (4096u + (unsigned)tok * 896u + 16u * ((lane & 7u) ^ sw) - 1024u * (j & 3));
  }
  c.sXo = 0u;
}
template <int STEP> DI void p1_W_step(P1& c, unsigned ldsb, int slot_off, unsigned w) {
  if constexpr (STEP == 0) setm0(ldsb + slot_off + w * 4096u);
  else if constexpr (STEP <= 4) dma16o<1024 * (STEP - 1)>(c.W.v[STEP - 1], c.W.srd, c.W.soff);
  else if constexpr (STEP == 5) setm0(ldsb + slot_off + 16384u + w * 2048u);
  else if constexpr (STEP == 6) dma16o<0>(c.vB[0], c.sB, c.sBo);
  else { dma16o<1024>(c.vB[1], c.sB, c.sBo); c.W.soff += c.W.gstep; c.sBo += 32u * 2688u; }
}
template <int STEP> DI void p1_x_step(P1& c, unsigned ldsb, int slot_off, unsigned w) {
  if constexpr (STEP == 0) setm0(ldsb + slot_off + w * 5120u);
  else if constexpr (STEP <= 4) dma16o<1024 * (STEP - 1)>(c.vX[STEP - 1], c.sX, c.sXo);
  else if constexpr (STEP == 5) setm0(ldsb + slot_off + w * 5120u + 4096u);
  else { dma16o<0>(c.vX[4], c.sX, c.sXo); c.sXo += 128u; }
}
template <int E4, int I> DI void p1_preamble(P1& c, srd_t sg, unsigned ldsb, unsigned w, unsigned lane) {
  if constexpr (I >= 4 && (I & 1) == 0) {
    constexpr int k = (I - 4) / 2;
    if constexpr (E4 == 1) {
      if constexpr (k < 7) p1_x_step<k>(c, ldsb, P1_XB + 0 * P1_XS, w);
      else if constexpr (k < 15) p1_W_step<k - 7>(c, ldsb, p1_wslot(0), w);
      else if constexpr (k < 23) p1_W_step<k - 15>(c, ldsb, p1_wslot(1), w);
    } else if constexpr (E4 == 2) {
      if constexpr (k < 7) p1_x_step<k>(c, ldsb, P1_XB + 1 * P1_XS, w);
    } else if constexpr (E4 == 3) {
      if constexpr (k == 0) setm0(ldsb + P1_G + w * 1024u);
      else if constexpr (k == 1) dma16o<0>(lane * 16u, sg, 0u);
      else if constexpr (k < 10) p1_W_step<k - 2>(c, ldsb, p1_wslot(2), w);
      else if constexpr (k < 17) p1_x_step<k - 10>(c, ldsb, P1_XB + 2 * P1_XS, w);
    }
  }
}

struct P1Out { float g[2]; u32x4 ob[2]; };

template <int NST, class St, class Done, class Early>
DI void p1_loop(P1& c, unsigned ldsb, unsigned w, unsigned lane, St st, Done done, Early early ST_ARG) {
  const unsigned r16 = lane & 15u, g = lane >> 4, q = (lane >> 2) & 3u, p = lane & 3u;
  unsigned bq[2][8], bb[2][6], bx[2];
  const unsigned fz = (q << 1) | (g & 1u);
#pragma unroll
  for (int s = 0; s < 2; ++s) {
#pragma unroll
    for (int n = 0; n < 8; ++n) bq[s][n] = ldsb + s * 2 * P1_WS + 512u * (8u * g + q) + 32u * ((unsigned)n ^ fz) + 8u * p;
#pragma unroll
    for (int n = 0; n < 6; ++n) bb[s][n] = ldsb + s * 2 * P1_WS + 16384u + 256u * (8u * g + q) + 32u * ((unsigned)n ^ fz) + 8u * p;
    bx[s] = ldsb + P1_XB + 128u * (32u * w + r16) + 16u * ((4u * s + g) ^ ((r16 >> 1) & 7u));
  }
  const unsigned bg = ldsb + P1_G + w * 1024u + 16u * g;
  u32x4 xa[2][3];
  auto rd_instr = [&](auto FF, auto II) {
    constexpr int F = FF, i = II, ks = F / 23, f = F % 23, sl = (ks + 2) % 4, set = sl / 2, so = (sl % 2) * P1_WS, reg = p1_reg(F);
    if constexpr (f < 16) tr_rd<reg + 2 * i, so + 256 * (f >> 3) + 2048 * i>(bq[set][f & 7]);
    else if constexpr (f < 22) tr_rd<reg + 2 * i, so + 1024 * i>(bb[set][f - 16]);
    else frag_rd<reg, 64 * ks>(bg);
  };
  auto rd_x = [&](auto KS, auto TB) { constexpr int ks = KS, tb = TB; lds_rdv<((ks >> 1) % 3) * P1_XS + 2048 * tb>(xa[ks & 1][tb], bx[ks & 1]); };
  waitv<34 - 11 + (NST == 2 ? 32 : 0)>();
  BAR();
  sfor<3>([&](auto TB) { rd_x(IC<0>{}, TB); });
  sfor<8>([&](auto FF) { rd_instr(FF, IC<0>{}); rd_instr(FF, IC<1>{}); });
  waitl<0>();
  pin(xa[0][0], xa[0][1], xa[0][2]);
  SB();
  sfor<14>([&](auto KS) {
    constexpr int ks = KS;
    STAMP(16 + ks);
    sfor<50>([&](auto GG) {
      constexpr int gg = GG, G = 50 * ks + gg;
      constexpr int f = gg < 36 ? gg / 2 : (gg < 48 ? 18 + (gg - 36) / 3 : 22), sub = gg - p1_use(f), F = 23 * ks + f, reg = p1_reg(F);
      constexpr bool Z = ks == 0;
      if constexpr (gg == 0 && ks > 0) {
        constexpr int n = p1_lds_between(p1_xrd(ks, 2), false, G);
        waitl<(n > 15 ? 15 : n)>();
        pin(xa[ks & 1][0], xa[ks & 1][1], xa[ks & 1][2]);
      }
      if constexpr (sub == 0 && (F % 2) == 0 && p1_rd(F) >= 0) {
        constexpr int FN = (F + 1 < P1_NF) ? F + 1 : F;
        constexpr int n = p1_lds_between(p1_rd(FN) + p1_ni(FN) - 1, true, G);
        static_assert(p1_rd(FN) + p1_ni(FN) <= G, "read too late");
        waitl<(n > 15 ? 15 : n)>();
      }
      if constexpr (f < 16) mfK<AO_(f, sub), reg, Z>(xa[ks & 1][1 + sub]);
      else if constexpr (f < 18) mfK<ABq(f - 16, sub), reg, Z>(xa[ks & 1][1 + sub]);
      else if constexpr (f < 20) mfK<ABk(f - 18, sub), reg, Z>(xa[ks & 1][sub]);
      else if constexpr (f < 22) mfV<ABv(f - 20, sub), reg, Z>(xa[ks & 1][sub]);
      else mfK<AZ(sub), reg, Z>(xa[ks & 1][1 + sub]);
      if constexpr (gg == P1_BAR && ks < 13) {
        constexpr int N = p1_wait(ks, NST);
        static_assert(N >= 0 && N < 63, "vmcnt range");
        if constexpr ((NST == 1 && ks == 11) || (NST == 2 && ks == KV_DONE_KS)) { waitv<0>(); BAR(); done(); }
        else { waitv<N>(); BAR(); }
      }
      sfor<12>([&](auto DD) {
        constexpr int H = 23 * ks + f + (int)DD;
        if constexpr (H < P1_NF) {
          if constexpr (p1_rd(H) == G) rd_instr(IC<H>{}, IC<0>{});
          else if constexpr (p1_ni(H) == 2 && p1_rd(H) + 1 == G) rd_instr(IC<H>{}, IC<1>{});
        }
      });
      if constexpr (ks < 13) { sfor<3>([&](auto TB) { if constexpr (p1_xrd(ks + 1, TB) == G) rd_x(IC<ks + 1>{}, TB); }); }
      if constexpr (ks + 3 < 14) {
        constexpr int so = p1_wslot(ks + 3);
        if constexpr (gg == 23) setm0(ldsb + so + w * 4096u);
        if constexpr (gg == 24) dma16o<0>(c.W.v[0], c.W.srd, c.W.soff);
        if constexpr (gg == 26) dma16o<1024>(c.W.v[1], c.W.srd, c.W.soff);
        if constexpr (gg == 27) dma16o<2048>(c.W.v[2], c.W.srd, c.W.soff);
        if constexpr (gg == 29) dma16o<3072>(c.W.v[3], c.W.srd, c.W.soff);
        if constexpr (gg == 30) setm0(ldsb + so + 16384u + w * 2048u);
        if constexpr (gg == 31) dma16o<0>(c.vB[0], c.sB, c.sBo);
        if constexpr (gg == 33) dma16o<1024>(c.vB[1], c.sB, c.sBo);
        if constexpr (gg == 34) { c.W.soff += c.W.gstep; c.sBo += 32u * 2688u; }
      }
      if constexpr (p1_xiss(ks)) {
        constexpr int so = P1_XB + (((ks + 5) / 2) % 3) * P1_XS;
        if constexpr (gg == 35) setm0(ldsb + so + w * 5120u);
        if constexpr (gg == 36) dma16o<0>(c.vX[0], c.sX, c.sXo);
        if constexpr (gg == 38) dma16o<1024>(c.vX[1], c.sX, c.sXo);
        if constexpr (gg == 39) dma16o<2048>(c.vX[2], c.sX, c.sXo);
        if constexpr (gg == 41) dma16o<3072>(c.vX[3], c.sX, c.sXo);
        if constexpr (gg == 42) setm0(ldsb + so + w * 5120u + 4096u);
        if constexpr (gg == 43) dma16o<0>(c.vX[4], c.sX, c.sXo);
        if constexpr (gg == 44) c.sXo += 128u;
      }
      if constexpr (NST == 1) {
        constexpr int b0 = p1_st0(ks), nb = p1_nb(ks), na = p1_na(ks);
        if constexpr (gg == 4 && nb >= 1) st(IC<(b0 < 32 ? b0 : 0)>{});
        if constexpr (gg == 14 && nb >= 2) st(IC<(b0 + 1 < 32 ? b0 + 1 : 0)>{});
        if constexpr (gg == 38 && na >= 3) st(IC<(b0 + nb < 32 ? b0 + nb : 0)>{});
        if constexpr (gg == 43 && na >= 2) st(IC<(b0 + nb + (na >= 3 ? 1 : 0) < 32 ? b0 + nb + (na >= 3 ? 1 : 0) : 0)>{});
        if constexpr (gg == 48 && na >= 1) st(IC<(b0 + nb + na - 1 < 32 && na >= 1 ? b0 + nb + na - 1 : 0)>{});
      }
      if constexpr (ks == 13 && gg == 24) early();
      SB();
    });
  });
  STAMP(30);
}

struct P1B { u32x4 kf[3], vf[2][2], qf[2]; float omg[2]; };
DI void p1_post_b1(P1B& B, P1Out& o, unsigned lane) {
  const unsigned g = lane >> 4;
  float (&omg)[2] = B.omg;
#pragma unroll
  for (int qb = 0; qb < 2; ++qb) {
    float f[4];
    if (qb == 0) acc_rd4n<AZ(0)>(f); else acc_rd4n<AZ(1)>(f);
    const float e = __builtin_amdgcn_exp2f(-LOG2E * f[0]);
    o.g[qb] = __builtin_amdgcn_rcpf(1.f + e);
    omg[qb] = 1.f - o.g[qb];
  }
  {
    u32x4 (&kf)[3] = B.kf; u32x4 (&vf)[2][2] = B.vf; u32x4 (&qf)[2] = B.qf;
    sfor<3>([&](auto TB) { kf[TB] = acc_pack1n<ABk(0, TB), ABk(1, TB)>(); });
    sfor<2>([&](auto QB) {
      qf[QB] = acc_packn<ABq(0, QB), ABq(1, QB)>(BSCALE);
      if (g == 3u) { qf[QB][2] = 0u; qf[QB][3] = 0u; }
      sfor<2>([&](auto D) { vf[QB][D] = acc_pack1n<ABv(D, QB), ABv(D, QB + 1)>(); });
    });
  }
}
DI void p1_post_b2(P1B& B, P1Out& o, int T, unsigned w, unsigned lane) {
  const unsigned r16 = lane & 15u, g = lane >> 4;
  const int T0 = 128 * T + 32 * (int)w;
  {
    u32x4 (&kf)[3] = B.kf; u32x4 (&vf)[2][2] = B.vf; u32x4 (&qf)[2] = B.qf; float (&omg)[2] = B.omg;
    u32x4 ones = {0x3f803f80u, 0x3f803f80u, 0x3f803f80u, 0x3f803f80u};
    f32x4 cin[2][2], s[2][2];
#pragma unroll
    for (int qb = 0; qb < 2; ++qb)
#pragma unroll
      for (int kk = 0; kk < 2; ++kk)
#pragma unroll
        for (int r = 0; r < 4; ++r) {
          const int diff = (int)r16 - 4 * (int)g - r + 16 * (1 - kk);
          const int key = T0 + 16 * qb + (int)r16 - diff;
          cin[qb][kk][r] = (diff >= 0 && diff <= 14 && (diff & 1) == 0 && key >= 0) ? 0.f : -__builtin_inff();
        }
    pin(cin[0][0], cin[0][1], cin[1][0], cin[1][1], qf[0], qf[1], kf[0], kf[1], kf[2], ones); snop<1>();
    mfma_vvc(s[0][0], kf[0], qf[0], cin[0][0]); mfma_vvc(s[0][1], kf[1], qf[0], cin[0][1]);
    mfma_vvc(s[1][0], kf[1], qf[1], cin[1][0]); mfma_vvc(s[1][1], kf[2], qf[1], cin[1][1]);
    snop<9>(); pin(s[0][0], s[0][1], s[1][0], s[1][1]);
    u32x4 pf[2];
#pragma unroll
    for (int qb = 0; qb < 2; ++qb) {
      float m = fmaxf(fmaxf(fmaxf(s[qb][0][0], s[qb][0][1]), fmaxf(s[qb][0][2], s[qb][0][3])), fmaxf(fmaxf(s[qb][1][0], s[qb][1][1]), fmaxf(s[qb][1][2], s[qb][1][3])));
      m = xmax16_32(m);
      pf[qb][0] = cvtpk(__builtin_amdgcn_exp2f(s[qb][0][0] - m), __builtin_amdgcn_exp2f(s[qb][0][1] - m)); pf[qb][1] = cvtpk(__builtin_amdgcn_exp2f(s[qb][0][2] - m), __builtin_amdgcn_exp2f(s[qb][0][3] - m));
      pf[qb][2] = cvtpk(__builtin_amdgcn_exp2f(s[qb][1][0] - m), __builtin_amdgcn_exp2f(s[qb][1][1] - m)); pf[qb][3] = cvtpk(__builtin_amdgcn_exp2f(s[qb][1][2] - m), __builtin_amdgcn_exp2f(s[qb][1][3] - m));
    }
    f32x4 o0[2], o1[2], ls[2];
    pin(pf[0], pf[1], vf[0][0], vf[0][1], vf[1][0], vf[1][1]); snop<1>();
    mfma_vv0(o0[0], vf[0][0], pf[0]); mfma_vv0(o1[0], vf[0][1], pf[0]); mfma_vv0(ls[0], ones, pf[0]);
    mfma_vv0(o0[1], vf[1][0], pf[1]); mfma_vv0(o1[1], vf[1][1], pf[1]); mfma_vv0(ls[1], ones, pf[1]);
    snop<9>(); pin(o0[0], o1[0], ls[0], o0[1], o1[1], ls[1]);
#pragma unroll
    for (int qb = 0; qb < 2; ++qb) {
      const float sc = omg[qb] * __builtin_amdgcn_rcpf(ls[qb][0]);
      u32x4 ob; ob[0] = cvtpk(o0[qb][0] * sc, o0[qb][1] * sc); ob[1] = cvtpk(o0[qb][2] * sc, o0[qb][3] * sc); ob[2] = cvtpk(o1[qb][0] * sc, o1[qb][1] * sc); ob[3] = cvtpk(o1[qb][2] * sc, o1[qb][3] * sc);
      if (g == 3u) { ob[2] = 0u; ob[3] = 0u; }
      o.ob[qb] = ob;
    }
  }
}
template <class QSt> DI void p1_post_q(QSt qst) {
  sfor<16>([&](auto I) {
    constexpr int qb = I >> 3, ds = I & 7;
    const u32x4 v = acc_pack<AO_(2 * ds, qb), AO_(2 * ds + 1, qb)>(QSCALE);
    qst(I, v);
    SB();
  });
}
constexpr float THR = 8.f;
constexpr int AF_(int seq) { return 192 + 4 * (seq & 15); }

struct Att {
  f32x4 S[2][4][2];
  f32x4 CIN[4][2];
  u32x4 P[2][2];
  f32x4 LT[2];
  float l[2];
  u32x4 ones;
  float mref[2], fct[2], mx, mx2;
  unsigned kb[2], vb[2];
  unsigned vlane;
  srd_t srdK, srdV;
  bool pend;
};
template <int AF, int AQ> DI void qk_first(f32x4& s, const f32x4& c) {
  asm volatile("v_mfma_f32_16x16x32_bf16 %0, a[%c2:%c3], a[%c4:%c5], %1" : "=&v"(s) : "v"(c), "i"(AF), "i"(AF + 3), "i"(AQ), "i"(AQ + 3));
}
template <int AF, int AQ> DI void qk_next(f32x4& s) {
  asm volatile("v_mfma_f32_16x16x32_bf16 %0, a[%c1:%c2], a[%c3:%c4], %0" : "+v"(s) : "i"(AF), "i"(AF + 3), "i"(AQ), "i"(AQ + 3));
}
template <int AD, int AF> DI void pv_mfma(const u32x4& p) {
  asm volatile("v_mfma_f32_16x16x32_bf16 a[%c1:%c2], a[%c3:%c4], %0, a[%c1:%c2]" :: "v"(p), "i"(AD), "i"(AD + 3), "i"(AF), "i"(AF + 3));
}
DI void l_mfma0(f32x4& l, const u32x4& ones, const u32x4& p) {
  asm volatile("v_mfma_f32_16x16x32_bf16 %0, %1, %2, 0" : "=&v"(l) : "v"(ones), "v"(p));
}
DI void l_mfma1(f32x4& l, const u32x4& ones, const u32x4& p) {
  asm volatile("v_mfma_f32_16x16x32_bf16 %0, %1, %2, %0" : "+v"(l) : "v"(ones), "v"(p));
}
DI float vexp(float x) { float r = __builtin_amdgcn_exp2f(x); asm volatile("" : "+v"(r)); return r; }
DI float vmax3(float a, float b, float c) { float r = __builtin_elementwise_maximum(__builtin_elementwise_maximum(a, b), c); asm volatile("" : "+v"(r)); return r; }
DI unsigned vcvt(float lo, float hi) { unsigned r = cvtpk(lo, hi); asm volatile("" : "+v"(r)); return r; }

constexpr int rdgap(int seq) {
  if (seq < 10) return 2 * seq - 20;
  if (seq < 32) return 2 * (seq - 10);
  const int v = seq - 32;
  if (v < 4) return 52 + 2 * v;
  if (v == 4) return 64;
  if (v == 5) return 66;
  if (v < 22) return 64 + (v - 2);
  return 84 + 2 * (v - 22);
}
constexpr int usegap(int seq) { return seq < 32 ? 2 * seq : 68 + 2 * (seq - 32); }
constexpr int nafter(int seq, int g) {
  int n = 0;
  for (int s = 0; s < 64; ++s) for (int p = -1; p <= 1; ++p) { const int r = rdgap(s) + 132 * p; if (r > rdgap(seq) && r < g) ++n; }
  return n;
}
constexpr int WG = 4;
template <int G> DI void maybe_wait() {
  constexpr int seq = (G < 64) ? ((G & 1) == 0 ? G / 2 : -1) : (G >= 68 && (G & 1) == 0 ? 32 + (G - 68) / 2 : -1);
  if constexpr (seq >= 0 && (seq % WG) == 0) {
    constexpr int n = nafter(seq + WG - 1, G);
    static_assert(rdgap(seq + WG - 1) < G, "fragment not yet requested");
    waitl<(n > 15 ? 15 : n)>();
  }
}
template <int G, int X> DI void do_reads(Att& c) {
  sfor<64>([&](auto S_) {
    constexpr int s = S_;
    if constexpr (rdgap(s) == G) {
      if constexpr (s < 32) frag_rd<AF_(s), s * 1024>(c.kb[X]); else frag_rd<AF_(s), (s - 32) * 1024>(c.vb[X ^ 1]);
    } else if constexpr (rdgap(s) + 132 == G) frag_rd<AF_(s), s * 1024>(c.kb[X ^ 1]);
  });
}

template <int X, int H, int E> DI void do_exp(Att& c) {
  constexpr int kb = 2 * H + (E >> 3), qb = (E >> 2) & 1, r = E & 3; c.S[X][kb][qb][r] = vexp(c.S[X][kb][qb][r]);
}
template <int X, int KS, int PP> DI void do_pack(Att& c) {
  constexpr int kbl = PP >> 2, qb = (PP >> 1) & 1, h = PP & 1, kb = 2 * KS + kbl;
  c.P[qb][KS][2 * kbl + h] = vcvt(c.S[X][kb][qb][2 * h], c.S[X][kb][qb][2 * h + 1]);
}
template <int X, int I> DI void do_max(Att& c) {
  constexpr int ch = I & 1, st = I >> 1, base = 16 * ch;
  auto SV = [&](auto J) -> float { constexpr int j = J; return c.S[X][j >> 3][(j >> 2) & 1][j & 3]; };
  float& m = ch ? c.mx2 : c.mx;
  if constexpr (st == 0) m = vmax3(SV(std::integral_constant<int, base>{}), SV(std::integral_constant<int, base + 1>{}), SV(std::integral_constant<int, base + 2>{}));
  else if constexpr (st < 7) m = vmax3(m, SV(std::integral_constant<int, base + 2 * st + 1>{}), SV(std::integral_constant<int, base + 2 * st + 2>{}));
  else if constexpr (ch == 0) m = vmax3(m, SV(std::integral_constant<int, base + 15>{}), SV(std::integral_constant<int, base + 15>{}));
  else m = vmax3(m, SV(std::integral_constant<int, base + 15>{}), c.mx);
}
template <int X, bool FIRSTT> DI void raise_max(Att& c) {
  asm volatile("s_nop 7" ::: "memory");
#pragma unroll
  for (int qb = 0; qb < 2; ++qb) {
    float m = c.S[X][0][qb][0];
#pragma unroll
    for (int kb = 0; kb < 4; ++kb)
#pragma unroll
      for (int r = 0; r < 4; ++r) m = fmaxf(m, c.S[X][kb][qb][r]);
    m = xmax16_32(m);
    float dl = FIRSTT ? fmaxf(m, -1e30f) : fmaxf(m, 0.f);
    c.mref[qb] += dl;
    c.fct[qb] = FIRSTT ? 1.f : c.fct[qb] * __builtin_amdgcn_exp2f(-dl);
#pragma unroll
    for (int kb = 0; kb < 4; ++kb)
#pragma unroll
      for (int r = 0; r < 4; ++r) c.S[X][kb][qb][r] -= dl;
  }
  if (!FIRSTT) c.pend = true;
}
DI void build_cin(Att& c, bool diag, int dq) {
  if (diag) {
#pragma unroll
    for (int kb = 0; kb < 4; ++kb)
#pragma unroll
      for (int qb = 0; qb < 2; ++qb)
#pragma unroll
        for (int r = 0; r < 4; ++r) c.CIN[kb][qb][r] = (16 * kb + r - 16 * qb > dq) ? -__builtin_inff() : -c.mref[qb];
  } else {
#pragma unroll
    for (int kb = 0; kb < 4; ++kb)
#pragma unroll
      for (int qb = 0; qb < 2; ++qb)
#pragma unroll
        for (int r = 0; r < 4; ++r) c.CIN[kb][qb][r] = -c.mref[qb];
  }
  asm volatile("s_nop 1" ::: "memory");
}
template <int A> DI void acc_scale(float f) {
  float t; asm volatile("v_accvgpr_read_b32 %0, a%c2\n\tv_mul_f32 %0, %0, %1\n\tv_accvgpr_write_b32 a%c2, %0" : "=&v"(t) : "v"(f), "i"(A) : ALL_AGPRS);
}
template <int A, int OFF, int AUX> DI void ld_acc16(unsigned voff, srd_t srd, unsigned soff) {
  asm volatile("s_nop 4\n\tbuffer_load_dwordx4 a[%c3:%c4], %0, %1, %2 offen offset:%c5 sc1" :: "v"(voff), "s"(srd), "s"(soff), "i"(A), "i"(A + 3), "i"(OFF) : "memory");
}
template <int A, int OFF> DI void ld_acc16n(unsigned voff, srd_t srd, unsigned soff) {
  asm volatile("buffer_load_dwordx4 a[%c3:%c4], %0, %1, %2 offen offset:%c5 sc1" :: "v"(voff), "s"(srd), "s"(soff), "i"(A), "i"(A + 3), "i"(OFF) : "memory");
}
template <int OFF> DI void ld_f32c(float& r, unsigned voff, srd_t srd) { asm volatile("buffer_load_dword %0, %1, %2, 0 offen offset:%c3 sc1" : "=v"(r) : "v"(voff), "s"(srd), "i"(OFF) : "memory"); }
DI void rescale_O(Att& c) {
  asm volatile("s_nop 7" ::: "memory");
  sfor<128>([&](auto I) { constexpr int a = I; acc_scale<a>(c.fct[(a >> 2) & 1]); });
  c.l[0] *= c.fct[0]; c.l[1] *= c.fct[1];
  asm volatile("s_nop 1" ::: "memory");
  c.fct[0] = 1.f; c.fct[1] = 1.f; c.pend = false;
}
template <int G8> DI void dmaK(Att& c, unsigned soff) { dma16c<1024 * (G8 & 3)>(c.vlane, c.srdK, (G8 >> 2) ? soff + 4096u : soff); }
template <int G8> DI void dmaKs(Att& c, unsigned soff) { dma16cs<1024 * (G8 & 3)>(c.vlane, c.srdK, (G8 >> 2) ? soff + 4096u : soff); }
template <int G8> DI void dmaVs(Att& c, unsigned soff) { dma16cs<1024 * (G8 & 3)>(c.vlane, c.srdV, (G8 >> 2) ? soff + 4096u : soff); }
template <int G8> DI void dmaV(Att& c, unsigned soff) { dma16c<1024 * (G8 & 3)>(c.vlane, c.srdV, (G8 >> 2) ? soff + 4096u : soff); }

template <int X, bool FIRST, class Mid> DI void phaseA(Att& c, Mid mid) {
  sfor<64>([&](auto G) {
    constexpr int g = G, k = g >> 1, qb = g & 1, kb = k >> 3, ds = k & 7;
    maybe_wait<g>();
    if constexpr (ds == 0) qk_first<AF_(k), AQ_(qb, 0)>(c.S[X][kb][qb], c.CIN[kb][qb]);
    else qk_next<AF_(k), AQ_(qb, ds)>(c.S[X][kb][qb]);
    if constexpr (g == 50) { waitvl<0>(); BAR(); mid(); }
    if constexpr (!FIRST || g < 50) do_reads<g, X>(c);
    if constexpr (!FIRST) {
      if constexpr ((g & 1) == 1 && g <= 31) do_exp<X ^ 1, 1, (g >> 1)>(c);
      if constexpr ((g & 1) == 0 && g <= 14) do_pack<X ^ 1, 0, (g >> 1)>(c);
      if constexpr ((g & 1) == 0 && g >= 34 && g <= 48) do_pack<X ^ 1, 1, ((g - 34) >> 1)>(c);
    }
    SB();
  });
}
template <int X, bool FIRST> DI void phaseB(Att& c, unsigned m0k, unsigned ksoff, unsigned m0v, unsigned vsoff) {
  sfor<68>([&](auto G) {
    constexpr int g = G;
    if constexpr (!FIRST) {
      maybe_wait<64 + g>();
      if constexpr (g < 2) l_mfma0(c.LT[g & 1], c.ones, c.P[g & 1][0]);
      else if constexpr (g < 4) l_mfma1(c.LT[g & 1], c.ones, c.P[g & 1][1]);
      else { constexpr int v = (g - 4) >> 1, qb = g & 1, db = v >> 1, ks = v & 1; pv_mfma<AO_(db, qb), AF_(32 + v)>(c.P[qb][ks]); }
    }
    if constexpr (FIRST && g == 0) asm volatile("s_nop 7" ::: "memory");
    if constexpr (!FIRST || g > 46) do_reads<64 + g, X>(c);
    if constexpr (!FIRST) {
      if constexpr (g == 40) { pin(c.LT[0]); c.l[0] += c.LT[0][0]; pin(c.l[0]); }
      if constexpr (g == 42) { pin(c.LT[1]); c.l[1] += c.LT[1][0]; pin(c.l[1]); }
      if constexpr ((g & 1) == 1 && g <= 31) do_max<X, (g >> 1)>(c);
    }
    if constexpr (g == 0) setm0(m0v);
    if constexpr (g == 1 || g == 3 || g == 21 || g == 23) { constexpr int j = g == 1 ? 0 : (g == 3 ? 1 : (g == 21 ? 2 : 3)); if constexpr (FIRST) dmaVs<j>(c, vsoff); else dmaV<j>(c, vsoff); }
    if constexpr (g == 23) setm0(m0v + 4096u);
    if constexpr (g == 25 || g == 27 || g == 29 || g == 31) { if constexpr (FIRST) dmaVs<4 + ((g - 25) >> 1)>(c, vsoff); else dmaV<4 + ((g - 25) >> 1)>(c, vsoff); }
    if constexpr (g == 33) setm0(m0k);
    if constexpr (g == 34 || g == 36 || g == 40 || g == 42) { constexpr int j = g == 34 ? 0 : (g == 36 ? 1 : (g == 40 ? 2 : 3)); if constexpr (FIRST) dmaKs<j>(c, ksoff); else dmaK<j>(c, ksoff); }
    if constexpr (g == 42) setm0(m0k + 4096u);
    if constexpr (g == 44 || g == 46 || g == 48 || g == 50) { if constexpr (FIRST) dmaKs<4 + ((g - 44) >> 1)>(c, ksoff); else dmaK<4 + ((g - 44) >> 1)>(c, ksoff); }
    if constexpr (g == 32) {
      if constexpr (FIRST) raise_max<X, true>(c);
      else { if (__builtin_expect(__any(c.mx2 > THR), 0)) raise_max<X, false>(c); }
    }
    if constexpr ((g & 1) == 1 && g >= 35 && g <= 65) do_exp<X, 0, ((g - 35) >> 1)>(c);
    SB();
  });
}
template <int X, int KIND, class Start, class Pre, class Step> DI void drain(Att& c, Start start, Pre pre, Step step) {
  start(IC<KIND>{});
  sfor<16>([&](auto I) { do_exp<X, 1, I>(c); });
  asm volatile("s_nop 0");
  sfor<8>([&](auto I) { do_pack<X, 0, I>(c); });
  sfor<8>([&](auto I) { do_pack<X, 1, I>(c); });
  waitvl<0>();
  BAR();
  pre(IC<KIND>{});
  sfor<12>([&](auto I) { frag_rd<AF_(32 + I), I * 1024>(c.vb[X]); });
  SB();
  sfor<68>([&](auto G) {
    constexpr int g = G;
    if constexpr (g >= 4 && (g & 1) == 0) { constexpr int v = (g - 4) >> 1; waitl<(v + 13 < 32 ? 13 : (31 - v))>(); }
    if constexpr (g < 2) l_mfma0(c.LT[g & 1], c.ones, c.P[g & 1][0]);
    else if constexpr (g < 4) l_mfma1(c.LT[g & 1], c.ones, c.P[g & 1][1]);
    else { constexpr int v = (g - 4) >> 1, qb = g & 1, db = v >> 1, ks = v & 1; pv_mfma<AO_(db, qb), AF_(32 + v)>(c.P[qb][ks]); }
    if constexpr ((g & 1) == 0 && g / 2 + 12 < 32) frag_rd<AF_(32 + g / 2 + 12), (g / 2 + 12) * 1024>(c.vb[X]);
    if constexpr (KIND != 0) step(IC<KIND>{}, G);
    if constexpr (g == 40) { pin(c.LT[0], c.LT[1]); c.l[0] += c.LT[0][0]; c.l[1] += c.LT[1][0]; pin(c.l[0], c.l[1]); }
    SB();
  });
}

struct P3 {
  srd_t srdW, srdP;
  unsigned vW[4];
  unsigned bw[3][4];
  unsigned bB[4];
  unsigned vlane, m0s[4], sW, pbase;
  float f[8];
};
constexpr int A3(int par, int nb, int qb) { return 32 * par + 8 * nb + 4 * qb; }
constexpr int R3(int F) { return 208 + 4 * (F % 12); }
constexpr int P3_LA = 16;
template <int A> DI float acc_rd1() { float r; asm volatile("v_accvgpr_read_b32 %0, a%c1" : "=v"(r) : "i"(A)); return r; }
DI void p3_issue_tile(P3& c, unsigned m0v, unsigned soff) {
  setm0(m0v); nop1();
  dma16s<0>(c.vW[0], c.srdW, soff); dma16s<1024>(c.vW[1], c.srdW, soff); dma16s<2048>(c.vW[2], c.srdW, soff); dma16s<3072>(c.vW[3], c.srdW, soff);
  setm0(m0v + 4096u); nop1();
  const unsigned s2 = soff + 4u * 7168u;
  dma16s<0>(c.vW[0], c.srdW, s2); dma16s<1024>(c.vW[1], c.srdW, s2); dma16s<2048>(c.vW[2], c.srdW, s2); dma16s<3072>(c.vW[3], c.srdW, s2);
}
template <int S> DI void p3_tile_step(P3& c, unsigned m0v, unsigned soff) {
  if constexpr (S == 0) setm0(m0v);
  else if constexpr (S <= 4) dma16o<1024 * (S - 1)>(c.vW[S - 1], c.srdW, soff);
  else if constexpr (S == 5) setm0(m0v + 4096u);
  else dma16o<1024 * (S - 6)>(c.vW[S - 6], c.srdW, soff + 4u * 7168u);
}
template <int CG, int F, int I> DI void p3_rd(P3& c) {
  constexpr int nb = F / 9, u = F % 9, reg = R3(F), sl = CG & 3;
  if constexpr (u < 8) tr_rd<reg + 2 * I, (sl == 1 ? 32768 : 0) + 4096 * u + 2048 * I>(c.bw[sl < 2 ? 0 : sl - 1][nb]);
  else tr_rd<reg + 2 * I, 1024 * CG + 16384 * I>(c.bB[nb]);
}
template <int CG>
DI void p3_group(P3& c, const u32x4 (&C)[2][8], const u32x4 (&OB)[2]) {
  constexpr int PAR = CG & 1;
  unsigned k0 = 0, k1 = 0, k2 = 0;
  sfor<72>([&](auto GG) {
    constexpr int g = GG, F = g >> 1, qb = g & 1, nb = F / 9, u = F % 9, reg = R3(F);
    if constexpr (qb == 0 && (F % 2) == 0) waitl<(CG == 6 && g + 16 > 72) ? (72 - g - 4 > 0 ? 72 - g - 4 : 0) : 12>();
    if constexpr (u < 8) mfK<A3(PAR, nb, qb), reg, u == 0>(C[qb][u]); else mfK<A3(PAR, nb, qb), reg, false>(OB[qb]);
    if constexpr (g == 40 && CG < 6) {
      constexpr int N = (CG + 2 <= 6 ? 8 : 0) + (CG >= 1 ? 4 : 0) + (CG >= 2 ? 4 : 0);
      waitv<(CG < 2 ? 63 : N)>(); BAR();
    }
    { constexpr int t = g + P3_LA, Fr = t >> 1, ir = t & 1;
      if constexpr (Fr < 36) p3_rd<CG, Fr, ir>(c); else if constexpr (CG < 6) p3_rd<CG + 1, Fr - 36, ir>(c); }
    if constexpr (CG + 3 <= 6) {
      constexpr int sl = (CG + 3) & 3;
      const unsigned so = c.sW + 128u * (CG + 3);
      if constexpr (g == 42) setm0(c.m0s[sl]);
      if constexpr (g == 43) dma16o<0>(c.vW[0], c.srdW, so);
      if constexpr (g == 45) dma16o<1024>(c.vW[1], c.srdW, so);
      if constexpr (g == 47) dma16o<2048>(c.vW[2], c.srdW, so);
      if constexpr (g == 49) dma16o<3072>(c.vW[3], c.srdW, so);
      if constexpr (g == 50) setm0(c.m0s[sl] + 4096u);
      if constexpr (g == 51) dma16o<0>(c.vW[0], c.srdW, so + 4u * 7168u);
      if constexpr (g == 53) dma16o<1024>(c.vW[1], c.srdW, so + 4u * 7168u);
      if constexpr (g == 55) dma16o<2048>(c.vW[2], c.srdW, so + 4u * 7168u);
      if constexpr (g == 57) dma16o<3072>(c.vW[3], c.srdW, so + 4u * 7168u);
    }
    if constexpr (CG >= 1) {
      sfor<4>([&](auto SS) {
        constexpr int s = SS, q2 = s >> 1, h2 = s & 1, g0 = 2 + 9 * s;
        if constexpr (g >= g0 && g < g0 + 8) { constexpr int e = g - g0; c.f[e] = acc_rd1<A3(PAR ^ 1, 2 * h2 + (e >> 2), q2) + (e & 3)>(); }
        if constexpr (g == g0 + 4) { k0 = cvtpk(c.f[0], c.f[1]); pin(k0); }
        if constexpr (g == g0 + 5) { k1 = cvtpk(c.f[2], c.f[3]); pin(k1); }
        if constexpr (g == g0 + 7) { k2 = cvtpk(c.f[4], c.f[5]); pin(k2); }
        if constexpr (g == g0 + 8) { const u32x4 pk = {k0, k1, k2, cvtpk(c.f[6], c.f[7])};
          st16<16>(c.srdP, c.vlane, c.pbase + (unsigned)((CG - 1) * 2048 + q2 * 14 * 1024 + h2 * 1024), pk); }
      });
    }
    SB();
  });
}

constexpr size_t FL_GEN = 0, FL_START = 4096, FL_KV = 8192, FL_Q = 16384, FL_O = 49152, FL_P = 81920;
typedef __attribute__((address_space(1))) unsigned gu32;
DI void flag_set(char* base, unsigned byteoff, unsigned tag) { __hip_atomic_store((gu32*)(base + byteoff), tag, __ATOMIC_RELAXED, __HIP_MEMORY_SCOPE_AGENT); }
DI unsigned flag_get(char* base, unsigned byteoff) { return __hip_atomic_load((gu32*)(base + byteoff), __ATOMIC_RELAXED, __HIP_MEMORY_SCOPE_AGENT); }
DI void ld_flag(unsigned& r, const char* p) { asm volatile("s_nop 4\n\tglobal_load_dword %0, %1, off sc1" : "=v"(r) : "v"(p) : "memory"); }
DI void poll_all(char* base, unsigned byteoff, unsigned tag) {
  unsigned spins = 0;
  while (!__all(flag_get(base, byteoff) == tag)) { if (++spins > (1u << 22)) break; __builtin_amdgcn_s_sleep(1); }
}

__global__ __launch_bounds__(256, 1) void fused_main(const bf16_t* __restrict__ x, const bf16_t* __restrict__ ckv, const bf16_t* __restrict__ wq, const bf16_t* __restrict__ wuk,
                                                    const bf16_t* __restrict__ wuv, const bf16_t* __restrict__ wo, const bf16_t* __restrict__ wqkv, const bf16_t* __restrict__ woB,
                                                    const bf16_t* __restrict__ gw, char* __restrict__ ws, bf16_t* __restrict__ out) {
  extern __shared__ __attribute__((aligned(16))) char smem[];
  LAS char* lds = (LAS char*)smem;
  const unsigned ldsb = (unsigned)(size_t)lds;
  asm volatile("" ::: ALL_AGPRS);
  unsigned long long code0; unsigned codelen;
  asm volatile("s_getpc_b64 %0\nfused_code_begin:\n\ts_mov_b32 %1, fused_code_end-fused_code_begin" : "=s"(code0), "=s"(codelen));
  STAMP_DECL; STAMP(0);
  const int b = blockIdx.x, xcd = b & 7, i = b >> 3;
  const int head = 2 * xcd + (i & 1);
  const unsigned lane = threadIdx.x & 63u, w = rfl(threadIdx.x >> 6);
  char* fl = ws + WS_FLAG;
  const srd_t srdH = mksrd(ws + (size_t)head * (1u << 20), 100u << 20);
  constexpr unsigned HQ = (unsigned)WS_Q, HPO = (unsigned)WS_PO;
  const unsigned hPART = (unsigned)WS_PART - (unsigned)head * (1u << 20);
  const unsigned hLSE = (unsigned)WS_LSE - (unsigned)head * (1u << 20);
  const unsigned gen = *(const __attribute__((address_space(4))) unsigned*)(fl + FL_GEN);
  const int isV = (i >> 1) & 1, tt = i >> 2;
  const unsigned tag = gen * 1664525u + 1013904223u;
  u32x4 kvp[32];
  P1 p1;
  const int pi = (i >> 1) & 7, role = i >> 4;
  constexpr int NA = NA_TILES;
  const int tl = 15 - pi, nl = 2 * (tl + 1);
  const int town = role ? pi : tl;
  {
    const unsigned wr = w >> 1, wc = w & 1u;
    XStream X; WStream W;
    X.srd = mksrd(ckv, SEQ * DC * 2);
#pragma unroll
    for (int j = 0; j < 8; ++j) X.v[j] = (8u * j + (lane >> 3)) * 2048u + 16u * ((lane & 7u) ^ (4u * (j & 1) + (lane >> 4))) - 1024u * (j & 3);
    X.soff = (256u * tt + 64u * w) * 2048u;
    makeW4(W, isV ? wuv : wuk, DC * 4096 * 2, 8192, head * 256, w, lane);
    unsigned xb[2], wb[8];
    {
      const unsigned r = lane & 15u, g = lane >> 4, q = (lane >> 2) & 3u, p = lane & 3u;
#pragma unroll
      for (int e = 0; e < 2; ++e) xb[e] = 128u * (128u * wr + r) + 16u * ((4u * e + g) ^ ((r >> 1) & 7u));
#pragma unroll
      for (int n = 0; n < 8; ++n) wb[n] = WBASE + 512u * (8u * g + q) + 32u * ((8u * wc + n) ^ ((q << 1) | (g & 1u))) + 8u * p;
    }
    issueX(X, ldsb + w * 8192u, 0);
    issueW(W, ldsb + WBASE + 0 * WSLOT + w * 4096u); issueX(X, ldsb + w * 8192u, 1);
    issueW(W, ldsb + WBASE + 1 * WSLOT + w * 4096u); issueX(X, ldsb + XSLOT + w * 8192u, 0);
    issueW(W, ldsb + WBASE + 2 * WSLOT + w * 4096u); issueX(X, ldsb + XSLOT + w * 8192u, 1);
    issueW(W, ldsb + WBASE + 3 * WSLOT + w * 4096u); issueX(X, ldsb + 2 * XSLOT + w * 8192u, 0);
    unsigned pj[1];
    { const srd_t scode = mksrd((const void*)code0, codelen);
      pf_load(pj[0], (w == 3u) ? ((unsigned)i * 64u + lane) * 128u : 0x7fffff00u, scode); }
    Prefetch pf;
    {
      const unsigned ci = (unsigned)i >> 1;
      pf.j0 = 0u; pf.so = 0u;
      if (w == 0) { pf.s0 = mksrd(wq, DM * 4096 * 2); pf.v0 = lane < 8u ? (28u * ci + (lane >> 2)) * 8192u + head * 512u + (lane & 3u) * 128u : 0x7fffff00u; pf.step = 16384u; }
      else if (w == 1) { pf.s0 = mksrd(wo, 4096 * DM * 2); pf.v0 = lane < 8u ? (unsigned)(head * 256 + 16 * (int)ci) * 896u + lane * 128u : 0x7fffff00u; pf.step = 1024u; }
      else if (w == 2) { pf.s0 = mksrd(wqkv, DM * 1344 * 2); pf.v0 = lane < 3u ? ((unsigned)b * 37u + lane) * 128u : 0x7fffff00u; pf.step = 384u; }
      else { pf.s0 = mksrd(x, SEQ * DM * 2); pf.v0 = lane < 4u ? ((unsigned)b * 56u + lane) * 128u : 0x7fffff00u; pf.step = 512u; }
    }
    { const u32x4 z = zero4(); nop1(); nop1(); sfor<16>([&](auto I) { clear16<16 * I>(z); }); }
    waitv<25>(); BAR();
    STAMP(1);
    Frags f0, f1;
#pragma unroll
    for (int n = 0; n < 8; ++n) { f0.wl[n] = readWh(lds, wb[n], 0); f0.wh[n] = readWh(lds, wb[n], 2048); }
#pragma unroll
    for (int m = 0; m < 8; ++m) f0.x[m] = readX(lds, xb[0], 2048 * m);
    f0.join();
    p1_setup(p1, x, wq, wqkv, head, town, w, lane);
    const srd_t sg = mksrd(gw, DM * 2);
    auto hook = [&](auto E4, auto I) { p1_preamble<decltype(E4)::value, decltype(I)::value>(p1, sg, ldsb, w, lane); };
    if (isV) kv_loop<true>(f0, f1, lds, ldsb, X, W, xb, wb, w, pf, hook);
    else kv_loop<false>(f0, f1, lds, ldsb, X, W, xb, wb, w, pf, hook);
    STAMP(2);
    pin(pf.j0); pin(pj[0]);
    snop<7>();
    if (isV) sfor<32>([&](auto I) { constexpr int mp = I / 8, n = I % 8; kvp[I] = acc_pack1n<4 * (8 * (2 * mp) + n), 4 * (8 * (2 * mp + 1) + n)>(); SB(); });
    else sfor<32>([&](auto I) { constexpr int m = I / 4, np = I % 4; kvp[I] = acc_pack1n<4 * (8 * m + 2 * np), 4 * (8 * m + 2 * np + 1)>(); SB(); });
    if (threadIdx.x == 0) flag_set(fl, FL_START + b * 4u, tag);
    STAMP(3);
  }
  P1Out po;
  unsigned kvflag = 0u;
  Att c;
  c.vlane = lane * 16u;
  {
    const unsigned wr = w >> 1, wc = w & 1u;
    const srd_t so = srdH;
    const unsigned base = (isV ? (unsigned)WS_V : 0u) + (4u * tt + 2u * wr) * 32768u + wc * (isV ? 16384u : 4096u);
    auto st = [&](auto I) {
      constexpr int ii = I;
      const unsigned off = isV ? (unsigned)(((ii / 8) >> 1) * 32768 + (ii % 8) * 2048 + ((ii / 8) & 1) * 1024) : (unsigned)(((ii / 4) >> 2) * 32768 + ((ii / 4) & 3) * 8192 + (ii % 4) * 1024);
      st16<16>(so, c.vlane, base + off, kvp[ii]);
    };
    auto done = [&] { if (lane == 0) flag_set(fl, FL_KV + (unsigned)(head * 64 + (i >> 1) * 4 + (int)w) * 4u, tag); };
    auto early = [&] { ld_flag(kvflag, fl + FL_KV + (unsigned)(head * 64) * 4u + lane * 4u); };
    p1_loop<1>(p1, ldsb, w, lane, st, done, early ST_PASS);
  }
  STAMP(4);
  const bool split = nl > NA;
  const int nseg = role ? 2 : 1, seg0 = (role && !split) ? 1 : 0;
  const int k01 = role ? NA : 0, n1 = role ? (split ? nl - NA : 0) : (split ? NA : nl), n2 = role ? 2 * (pi + 1) : 0;
  c.srdK = mksrd(ws + WS_K + (size_t)head * (1u << 20), 1u << 20);
  c.srdV = mksrd(ws + WS_V + (size_t)head * (1u << 20), 1u << 20);
  { u32x4 o = {0x3f803f80u, 0x3f803f80u, 0x3f803f80u, 0x3f803f80u}; asm volatile("" : "+v"(o)); c.ones = o; }
  auto toff = [&](int jj) -> unsigned {
    unsigned t = (jj < n1) ? (unsigned)(k01 + jj) : (unsigned)(jj - n1);
    unsigned o = t * 32768u + w * 8192u;
    return (jj < n1 + n2) ? o : 0x40000000u;
  };
  const unsigned mK = ldsb + w * 8192u, mV = ldsb + 65536u + w * 8192u;
  auto dmaK8 = [&](int jj) { const unsigned so = toff(jj), m0 = mK + (jj & 1) * 32768u;
    setm0(m0); nop1(); sfor<4>([&](auto I) { dmaKs<I>(c, so); }); setm0(m0 + 4096u); nop1(); sfor<4>([&](auto I) { dmaKs<4 + I>(c, so); }); };
  auto dmaV8 = [&](int jj) { const unsigned so = toff(jj), m0 = mV + (jj & 1) * 32768u;
    setm0(m0); nop1(); sfor<4>([&](auto I) { dmaVs<I>(c, so); }); setm0(m0 + 4096u); nop1(); sfor<4>([&](auto I) { dmaVs<4 + I>(c, so); }); };
  auto loadQ = [&](int qt) {
    const unsigned so = HQ + (unsigned)(8 * qt + 2 * w) * 8192u;
    sfor<16>([&](auto I) { constexpr int qb = I >> 3, ds = I & 7; ld_acc16<AQ_(qb, ds), (I & 3) * 1024, 16>(c.vlane, srdH, so + (unsigned)(I >> 2) * 4096u); });
  };
  const unsigned fQ = FL_Q + (unsigned)((head * 8 + pi) * 4 + (int)w) * 64u, fO = FL_O + (unsigned)((head * 8 + pi) * 4 + (int)w) * 64u;
  auto poll1 = [&](unsigned off) { unsigned spins = 0; while (rfl(flag_get(fl, off)) != tag) { if (++spins > (1u << 22)) break; __builtin_amdgcn_s_sleep(1); } };
  waitv<0>(); pin(kvflag);
  if (!__all(kvflag == tag)) poll_all(fl, FL_KV + (unsigned)(head * 64) * 4u + lane * 4u, tag);
  BAR();
  dmaK8(0); dmaK8(1);
  STAMP(47);
  P1B pb;
  snop<7>(); snop<3>();
  p1_post_b1(pb, po, lane);
  {
    const unsigned so = HQ + (unsigned)(8 * town + 2 * (int)w) * 8192u;
    p1_post_q([&](auto I, const u32x4& v) { acc_wr4<AQ_(I >> 3, I & 7)>(v); st16<16>(srdH, c.vlane + (I & 3) * 1024u, so + (unsigned)(I >> 2) * 4096u, v); });
  }
  snop<1>();
  { const u32x4 z = zero4(); nop1(); nop1(); sfor<8>([&](auto I) { clear16<16 * I>(z); }); }
  STAMP(5);
  p1_post_b2(pb, po, town, w, lane);
  STAMP(45);
  waitv<0>();
  if (!role && split && lane == 0) flag_set(fl, fQ, tag);
  STAMP(44);
  if (role && split) { poll1(fQ); loadQ(tl); }
  STAMP(6);
  P3 p3;
  int jb = 0;
  bool pendO = false;
  for (int seg = seg0; seg < nseg; ++seg) {
    const int qt = seg ? town : tl, k0 = seg ? 0 : k01, n = seg ? n2 : n1;
    const bool lastseg = seg + 1 == nseg;
    const int q0 = 128 * qt + 32 * w;
    const unsigned par = jb & 1;
    c.kb[0] = ldsb + c.vlane + par * 32768u; c.kb[1] = ldsb + c.vlane + (par ^ 1) * 32768u;
    c.vb[0] = ldsb + c.vlane + 65536u + par * 32768u; c.vb[1] = ldsb + c.vlane + 65536u + (par ^ 1) * 32768u;
    c.l[0] = c.l[1] = 0.f;
    c.mref[0] = c.mref[1] = 0.f; c.fct[0] = c.fct[1] = 1.f; c.pend = false;
    const int dqb = q0 + (int)(lane & 15u) - 4 * (int)(lane >> 4);
    auto isdiag = [&](int t) { return (k0 + t) >= 2 * qt; };
    build_cin(c, isdiag(0), dqb - 64 * k0);
    if (seg == seg0) { waitv<0>(); BAR(); }
    sfor<10>([&](auto I) { frag_rd<AF_(I), I * 1024>(c.kb[0]); });
    waitl<0>();
    SB();
    const unsigned so = HPO + (unsigned)(8 * qt + 2 * w) * 8192u;
    float* lsep = (float*)(ws + WS_LSE) + head * 2048 + q0 + (lane & 15u);
    float lseB[2] = {0.f, 0.f};
    unsigned oflag = 0u, vbB = 0u, sW = 0u, sWB = 0u, m0B = 0u, lse_a = 0u;
    srd_t sB = p3.srdW;
    auto dstart = [&](auto K) { if constexpr (K == 2) ld_flag(oflag, fl + fO); };
    auto dpre = [&](auto K) {
      if constexpr (K != 0) {
        if constexpr (K == 2) { pin(oflag); if (rfl(oflag) != tag) poll1(fO); }
        const unsigned g = lane >> 4, q = (lane >> 2) & 3u, p = lane & 3u;
        p3.srdW = mksrd(wo, 4096 * DM * 2);
#pragma unroll
        for (int jj = 0; jj < 4; ++jj) p3.vW[jj] = (8u * jj + (lane >> 3)) * 896u + 16u * ((lane & 7u) ^ (((lane >> 4) & 3u) << 1)) - 1024u * jj;
        sW = (unsigned)(head * 256 + 64 * (int)w) * 896u;
        sB = mksrd((const char*)woB - 4096, DM * DM * 2 + 4096);
        vbB = 4096u + (lane >> 3) * 896u + 16u * ((lane & 7u) ^ (((lane >> 4) & 3u) << 1));
        m0B = mV + ((jb + n) & 1) * 32768u; sWB = (unsigned)(28 * head + 8 * (int)w) * 896u;
        const unsigned r = 4u * g + q, swz = (r >> 1) & 3u;
        const unsigned vlast = 65536u + ((jb + n + 1) & 1) * 32768u;
#pragma unroll
        for (int nb = 0; nb < 4; ++nb) {
          const unsigned b0 = ldsb + 128u * r + 32u * ((unsigned)nb ^ swz) + 8u * p;
          p3.bw[0][nb] = b0; p3.bw[1][nb] = b0 + 131072u; p3.bw[2][nb] = b0 + vlast;
          p3.bB[nb] = ldsb + 65536u + ((jb + n) & 1) * 32768u + (r >> 3) * 8192u + (r & 7u) * 128u + 32u * ((unsigned)nb ^ swz) + 8u * p;
        }
        p3.m0s[0] = mK; p3.m0s[1] = mK + 32768u; p3.m0s[2] = ldsb + 131072u + w * 8192u; p3.m0s[3] = ldsb + vlast + w * 8192u;
        p3.sW = sW;
        lse_a = hLSE + (unsigned)(head * 2048 + q0 + (int)(lane & 15u)) * 4u;
      }
    };
    auto dstep = [&](auto K, auto G) {
      constexpr int k0_ = (int)G - 1 - (K == 2 ? 18 : 0);
      if constexpr (K == 2 && G >= 1 && G <= 18) {
        constexpr int j = (int)G - 1;
        if constexpr (j < 2) ld_f32c<64 * j>(lseB[j], lse_a, srdH);
        else { constexpr int I = j - 2, qb = I >> 3, u = I & 7; ld_acc16n<AQ_(qb, u), (u & 3) * 1024>(c.vlane, srdH, so + (unsigned)(2 * qb + (u >> 2)) * 4096u); }
      } else if constexpr (k0_ >= 0 && k0_ < 39) {
        if constexpr (k0_ < 10) p3_tile_step<k0_>(p3, p3.m0s[0], sW);
        else if constexpr (k0_ < 19) {
          constexpr int s = k0_ - 10;
          if constexpr (s == 0) setm0(m0B);
          else if constexpr (s <= 4) dma16o<1024 * (s - 1)>(vbB + (unsigned)(128 * (s - 1) - 1024 * (s - 1)), sB, sWB);
          else if constexpr (s == 5) setm0(m0B + 4096u);
          else dma16o<1024 * (s - 6)>(vbB + (unsigned)(128 * (s - 2) - 1024 * (s - 6)), sB, sWB);
        }
        else if constexpr (k0_ < 29) p3_tile_step<k0_ - 19>(p3, p3.m0s[1], sW + 128u);
        else p3_tile_step<k0_ - 29>(p3, p3.m0s[2], sW + 256u);
      }
    };
    auto run_drain = [&](auto XX) {
      constexpr int X_ = XX;
      if (!lastseg) drain<X_, 0>(c, dstart, dpre, dstep);
      else if (!role && split) drain<X_, 2>(c, dstart, dpre, dstep);
      else drain<X_, 1>(c, dstart, dpre, dstep);
    };
    auto nomid = [] {};
    phaseA<0, true>(c, [&] { if (pendO) { if (lane == 0) flag_set(fl, fO, tag); pendO = false; } });
    phaseB<0, true>(c, mK + ((jb + 2) & 1) * 32768u, toff(jb + 2), mV + (jb & 1) * 32768u, toff(jb));
    if (1 < n) { build_cin(c, isdiag(1), dqb - 64 * (k0 + 1)); }
    int t = 1;
    for (;;) {
      if (t >= n) { run_drain(IC<0>{}); break; }
      phaseA<1, false>(c, nomid);
      if (t == n - 1 && !lastseg) loadQ(town);
      phaseB<1, false>(c, mK + ((jb + t) & 1) * 32768u, toff(jb + t + 2), mV + ((jb + t) & 1) * 32768u, toff(jb + t));
      { const bool nd = (t + 1 < n) && isdiag(t + 1);
        if (__builtin_expect(c.pend || nd, 0)) { if (c.pend) rescale_O(c); build_cin(c, nd, dqb - 64 * (k0 + t + 1)); } }
      ++t;
      if (t >= n) { run_drain(IC<1>{}); break; }
      phaseA<0, false>(c, nomid);
      if (t == n - 1 && !lastseg) loadQ(town);
      phaseB<0, false>(c, mK + ((jb + t) & 1) * 32768u, toff(jb + t + 2), mV + ((jb + t) & 1) * 32768u, toff(jb + t));
      { const bool nd = (t + 1 < n) && isdiag(t + 1);
        if (__builtin_expect(c.pend || nd, 0)) { if (c.pend) rescale_O(c); build_cin(c, nd, dqb - 64 * (k0 + t + 1)); } }
      ++t;
    }
    if (n == 1 && !lastseg) loadQ(town);
    STAMP_D(7 + seg);
    asm volatile("s_nop 7" ::: "memory");
    if (!lastseg) {
      float cA[2];
#pragma unroll
      for (int qb = 0; qb < 2; ++qb) {
        cA[qb] = c.l[qb] > 0.f ? __builtin_amdgcn_rcpf(c.l[qb]) : 0.f;
        const float lse = c.l[qb] > 0.f ? c.mref[qb] + __builtin_amdgcn_logf(c.l[qb]) : -1e30f;
        if (lane < 16u) __hip_atomic_store(lsep + 16 * qb, lse, __ATOMIC_RELAXED, __HIP_MEMORY_SCOPE_AGENT);
      }
      sfor<16>([&](auto I) {
        constexpr int qb = I >> 3, u = I & 7;
        st16<16>(srdH, c.vlane + (u & 3) * 1024u, so + (unsigned)(2 * qb + (u >> 2)) * 4096u, acc_pack<AO_(2 * u, qb), AO_(2 * u + 1, qb)>(cA[qb]));
        SB();
      });
      pendO = true;
      snop<1>();
      { const u32x4 z = zero4(); nop1(); nop1(); sfor<8>([&](auto I) { clear16<16 * I>(z); }); }
    } else {
      if (!role && split) { waitv<31>(); pin(lseB[0], lseB[1]); }
      float cA[2], cB[2] = {0.f, 0.f};
      if (!role && split) {
#pragma unroll
        for (int qb = 0; qb < 2; ++qb) {
          const float lseA = c.mref[qb] + __builtin_amdgcn_logf(c.l[qb]);
          const float M = fmaxf(lseA, lseB[qb]);
          const float wA = __builtin_amdgcn_exp2f(lseA - M), wB = __builtin_amdgcn_exp2f(lseB[qb] - M);
          const float it = po.g[qb] * __builtin_amdgcn_rcpf(wA + wB);
          cA[qb] = wA * it * __builtin_amdgcn_rcpf(c.l[qb]); cB[qb] = wB * it;
        }
      } else {
#pragma unroll
        for (int qb = 0; qb < 2; ++qb) cA[qb] = po.g[qb] * __builtin_amdgcn_rcpf(c.l[qb]);
      }
      u32x4 C[2][8];
      if (!role && split) {
        sfor<16>([&](auto I) {
          constexpr int qb = I >> 3, u = I & 7;
          float f0[4], f1[4], pb[4];
          acc_rd4n<AO_(2 * u, qb)>(f0); acc_rd4n<AO_(2 * u + 1, qb)>(f1); acc_rd4n<AQ_(qb, u)>(pb);
          float o[8];
#pragma unroll
          for (int r = 0; r < 4; ++r) { o[r] = f0[r] * cA[qb]; o[4 + r] = f1[r] * cA[qb]; }
#pragma unroll
          for (int d = 0; d < 4; ++d) {
            o[2 * d] += cB[qb] * __uint_as_float(__float_as_uint(pb[d]) << 16);
            o[2 * d + 1] += cB[qb] * __uint_as_float(__float_as_uint(pb[d]) & 0xffff0000u);
          }
          u32x4 v; v[0] = cvtpk(o[0], o[1]); v[1] = cvtpk(o[2], o[3]); v[2] = cvtpk(o[4], o[5]); v[3] = cvtpk(o[6], o[7]);
          C[qb][u] = v;
          SB();
        });
      } else {
        sfor<16>([&](auto I) { constexpr int qb = I >> 3, u = I & 7; C[qb][u] = acc_packn<AO_(2 * u, qb), AO_(2 * u + 1, qb)>(cA[qb]); SB(); });
      }
      STAMP(9);
      waitv<0>(); BAR();
      p3.srdP = srdH;
      p3.vlane = c.vlane;
      p3.pbase = hPART + (unsigned)head * (128u * 14u * 1024u) + (unsigned)(8 * qt + 2 * (int)w) * (14u * 1024u);
      sfor<16>([&](auto T) { p3_rd<0, (T >> 1), (T & 1)>(p3); });
      waitl<0>();
      sfor<16>([&](auto I) { pin(C[I >> 3][I & 7]); }); pin(po.ob[0], po.ob[1]); snop<1>();
      SB();
      STAMP(32);
      sfor<7>([&](auto CG) { p3_group<CG>(p3, C, po.ob); });
      asm volatile("s_nop 7" ::: "memory");
      sfor<4>([&](auto SS) {
        constexpr int q2 = SS >> 1, h2 = SS & 1;
        st16<16>(p3.srdP, p3.vlane, p3.pbase + 6u * 2048u + (unsigned)(q2 * 14 * 1024 + h2 * 1024), acc_pack1<A3(0, 2 * h2, q2), A3(0, 2 * h2 + 1, q2)>());
      });
    }
    jb += n;
  }
  waitv<0>();
  STAMP(10);
  if (lane == 0) flag_set(fl, FL_P + (unsigned)(town * 64 + head * 4 + (int)w) * 4u, tag);
  {
    const srd_t sp = srdH;
    u32x4 v[2][16];
    unsigned selLo = 0x00003f80u, selHi = 0x3f800000u; pin(selLo, selHi);
    const unsigned un0 = 7u * head + w, un1 = un0 + 4u;
    const unsigned o0 = hPART + ((8u * town + un0 / 14u) * 14u + un0 % 14u) * 1024u, o1 = (w < 3u) ? hPART + ((8u * town + un1 / 14u) * 14u + un1 % 14u) * 1024u : 0x40000000u;
    unsigned long long ready = __ballot(flag_get(fl, FL_P + (unsigned)(town * 64) * 4u + lane * 4u) == tag);
    unsigned spins = 0;
    float a[2][8] = {{0.f, 0.f, 0.f, 0.f, 0.f, 0.f, 0.f, 0.f}, {0.f, 0.f, 0.f, 0.f, 0.f, 0.f, 0.f, 0.f}};
    auto add = [&](int h) {
#pragma unroll
      for (int k = 0; k < 2; ++k)
#pragma unroll
        for (int d = 0; d < 4; ++d) { a[k][2 * d] = dot2bf(v[k][h][d], selLo, a[k][2 * d]); a[k][2 * d + 1] = dot2bf(v[k][h][d], selHi, a[k][2 * d + 1]); }
    };
#pragma unroll
    for (int h = 0; h < 16; ++h) {
      while (((ready >> (4 * h)) & 15ull) != 15ull) {
        if (++spins > (1u << 22)) break;
        __builtin_amdgcn_s_sleep(1);
        ready = __ballot(flag_get(fl, FL_P + (unsigned)(town * 64) * 4u + lane * 4u) == tag);
      }
      v[0][h] = __builtin_amdgcn_raw_buffer_load_b128(sp, lane * 16u, (unsigned)h * (128u * 14u * 1024u) + o0, 16);
      v[1][h] = __builtin_amdgcn_raw_buffer_load_b128(sp, lane * 16u, (unsigned)h * (128u * 14u * 1024u) + o1, 16);
      if (h >= 6) { SB(); add(h - 6); SB(); }
    }
    STAMP(48);
#pragma unroll
    for (int h = 10; h < 16; ++h) add(h);
#pragma unroll
    for (int k = 0; k < 2; ++k) {
      if (k == 1 && w == 3) break;
      const unsigned un = k ? un1 : un0, tb = 8u * town + un / 14u, u = un % 14u;
      bf16_t* o = out + (size_t)(16u * tb + (lane & 15u)) * DM + 32u * u + 4u * (lane >> 4);
      *(uint2*)o = make_uint2(cvtpk(a[k][0], a[k][1]), cvtpk(a[k][2], a[k][3]));
      *(uint2*)(o + 16) = make_uint2(cvtpk(a[k][4], a[k][5]), cvtpk(a[k][6], a[k][7]));
    }
  }
  if (b == 0 && w == 0) {
    for (int r = 0; r < 4; ++r) poll_all(fl, FL_START + (unsigned)(r * 64) * 4u + lane * 4u, tag);
    if (lane == 0) flag_set(fl, FL_GEN, tag);
  }
  STAMP(11); STAMP_OUT(1);
  asm volatile("fused_code_end:");
}

extern "C" void kernel_launch(void* const* d_in, const int* in_sizes, int n_in, void* d_out, int out_size, void* d_ws, size_t ws_size, hipStream_t stream) {
  static bool attr = false;
  if (!attr) { (void)hipFuncSetAttribute((const void*)fused_main, hipFuncAttributeMaxDynamicSharedMemorySize, LDS_BYTES); attr = true; }
  fused_main<<<256, 256, LDS_BYTES, stream>>>((const bf16_t*)d_in[0], (const bf16_t*)d_in[1], (const bf16_t*)d_in[2], (const bf16_t*)d_in[3], (const bf16_t*)d_in[4],
                                             (const bf16_t*)d_in[5], (const bf16_t*)d_in[7], (const bf16_t*)d_in[8], (const bf16_t*)d_in[9], (char*)d_ws, (bf16_t*)d_out);
}
```

```cpp
#include <hip/hip_runtime.h>
#include <stdint.h>
#include <utility>
#include <type_traits>

typedef unsigned short bf16_t;
typedef __bf16 bf16x8 __attribute__((ext_vector_type(8)));
typedef __bf16 bf16x4 __attribute__((ext_vector_type(4)));
typedef __bf16 bf16x2 __attribute__((ext_vector_type(2)));
typedef float f32x4 __attribute__((ext_vector_type(4)));
typedef float f32x2 __attribute__((ext_vector_type(2)));
typedef unsigned u32x4 __attribute__((ext_vector_type(4)));
typedef __amdgpu_buffer_rsrc_t srd_t;
#define LAS __attribute__((address_space(3)))
#define DI __device__ __forceinline__
#define SB() __builtin_amdgcn_sched_barrier(0)
#define ACL8(b) "a" #b "0","a" #b "1","a" #b "2","a" #b "3","a" #b "4","a" #b "5","a" #b "6","a" #b "7","a" #b "8","a" #b "9"
#define ALL_AGPRS "a0","a1","a2","a3","a4","a5","a6","a7","a8","a9", ACL8(1),ACL8(2),ACL8(3),ACL8(4),ACL8(5),ACL8(6),ACL8(7),ACL8(8),ACL8(9),ACL8(10),ACL8(11),ACL8(12), \
  ACL8(13),ACL8(14),ACL8(15),ACL8(16),ACL8(17),ACL8(18),ACL8(19),ACL8(20),ACL8(21),ACL8(22),ACL8(23),ACL8(24),"a250","a251","a252","a253","a254","a255"

#define KV_DONE_KS 5
#define KV_AUX 16
#define DMA_PLACE 0
#define NA_TILES 18
constexpr int SEQ = 2048, DM = 448, DC = 1024;
constexpr size_t WS_K = 0, WS_V = 16u << 20, WS_Q = 32u << 20, WS_PO = 48u << 20, WS_PART = 64u << 20, WS_LSE = 96u << 20, WS_FLAG = 97u << 20;
constexpr size_t WS_DBG = 100u << 20;
constexpr float LOG2E = 1.4426950408889634f;
constexpr float QSCALE = 0.0625f * LOG2E;
constexpr float BSCALE = 0.1889822365046136f * LOG2E;
constexpr int LDS_BYTES = 163840;

DI unsigned cvtpk(float lo, float hi) { f32x2 v = {lo, hi}; bf16x2 b = __builtin_convertvector(v, bf16x2); return __builtin_bit_cast(unsigned, b); }
DI float dot2bf(unsigned a, unsigned b, float c) { return __builtin_amdgcn_fdot2_f32_bf16(__builtin_bit_cast(bf16x2, a), __builtin_bit_cast(bf16x2, b), c, false); }
DI unsigned rfl(unsigned x) { return __builtin_amdgcn_readfirstlane(x); }
DI srd_t mksrd(const void* p, unsigned bytes) { return __builtin_amdgcn_make_buffer_rsrc((void*)p, (short)0, (int)bytes, 0x00020000); }
template <int... Is, class F> DI void sfor_impl(std::integer_sequence<int, Is...>, F&& f) { (f(std::integral_constant<int, Is>{}), ...); }
template <int N, class F> DI void sfor(F&& f) { sfor_impl(std::make_integer_sequence<int, N>{}, f); }
template <int N> using IC = std::integral_constant<int, N>;

template <int OFF> DI void dma16o(unsigned voff, srd_t srd, unsigned soff) {
  asm volatile("buffer_load_dwordx4 %0, %1, %2 offen offset:%c3 lds" :: "v"(voff), "s"(srd), "s"(soff), "i"(OFF) : "memory");
}
template <int OFF> DI void dma16s(unsigned voff, srd_t srd, unsigned soff) {
  asm volatile("s_nop 4\n\tbuffer_load_dwordx4 %0, %1, %2 offen offset:%c3 lds" :: "v"(voff), "s"(srd), "s"(soff), "i"(OFF) : "memory");
}
template <int OFF> DI void dma16c(unsigned voff, srd_t srd, unsigned soff) {
  asm volatile("buffer_load_dwordx4 %0, %1, %2 offen offset:%c3 sc1 lds" :: "v"(voff), "s"(srd), "s"(soff), "i"(OFF) : "memory");
}
template <int OFF> DI void dma16cs(unsigned voff, srd_t srd, unsigned soff) {
  asm volatile("s_nop 4\n\tbuffer_load_dwordx4 %0, %1, %2 offen offset:%c3 sc1 lds" :: "v"(voff), "s"(srd), "s"(soff), "i"(OFF) : "memory");
}
DI void setm0(unsigned v) { asm volatile("s_mov_b32 m0, %0" :: "s"(v) : "memory"); }
DI void nop1() { asm volatile("s_nop 0"); }
template <int N> DI void waitv() { asm volatile("s_waitcnt vmcnt(%c0)" :: "i"(N) : "memory", ALL_AGPRS); }
template <int N> DI void waitvl() { asm volatile("s_waitcnt vmcnt(%c0) lgkmcnt(0)" :: "i"(N) : "memory", ALL_AGPRS); }
template <int N> DI void waitl() { asm volatile("s_waitcnt lgkmcnt(%c0)" :: "i"(N) : "memory"); }
template <class T> DI void pin(T& a) { asm volatile("" : "+v"(a)); }
template <class T, class... R> DI void pin(T& a, R&... r) { pin(a); pin(r...); }
template <int N> DI void snop() { asm volatile("s_nop %c0" :: "i"(N) : "memory"); }
#define BAR() __builtin_amdgcn_s_barrier()

#define STAMP_DECL
#define STAMP(i)
#define STAMP_D(i)
#define STAMP_OUT(k)
#define ST_ARG
#define ST_PASS

template <int AD> DI void clear16(const u32x4& z) {
  asm volatile("v_mfma_f32_32x32x16_bf16 a[%c1:%c2], %0, %0, 0" :: "v"(z), "i"(AD), "i"(AD + 15) : ALL_AGPRS);
}
DI u32x4 zero4() { unsigned a, b, c, e; asm volatile("s_nop 1\n\tv_mov_b32 %0, 0\n\tv_mov_b32 %1, 0\n\tv_mov_b32 %2, 0\n\tv_mov_b32 %3, 0" : "=v"(a), "=v"(b), "=v"(c), "=v"(e)); u32x4 z = {a, b, c, e}; asm volatile("" : "+v"(z)); return z; }
template <int A> DI void acc_rd4(float (&f)[4]) {
  asm volatile("s_nop 1\n\tv_accvgpr_read_b32 %0, a%c4\n\tv_accvgpr_read_b32 %1, a%c5\n\tv_accvgpr_read_b32 %2, a%c6\n\tv_accvgpr_read_b32 %3, a%c7"
               : "=v"(f[0]), "=v"(f[1]), "=v"(f[2]), "=v"(f[3]) : "i"(A), "i"(A + 1), "i"(A + 2), "i"(A + 3) : ALL_AGPRS);
}
template <int A> DI void acc_rd4n(float (&f)[4]) {
  asm volatile("v_accvgpr_read_b32 %0, a%c4\n\tv_accvgpr_read_b32 %1, a%c5\n\tv_accvgpr_read_b32 %2, a%c6\n\tv_accvgpr_read_b32 %3, a%c7"
               : "=v"(f[0]), "=v"(f[1]), "=v"(f[2]), "=v"(f[3]) : "i"(A), "i"(A + 1), "i"(A + 2), "i"(A + 3) : ALL_AGPRS);
}
template <int A, int B> DI u32x4 acc_pack1n() {
  float f[4], g[4]; acc_rd4n<A>(f); acc_rd4n<B>(g);
  u32x4 v; v[0] = cvtpk(f[0], f[1]); v[1] = cvtpk(f[2], f[3]); v[2] = cvtpk(g[0], g[1]); v[3] = cvtpk(g[2], g[3]);
  return v;
}
template <int A, int B> DI u32x4 acc_packn(float sc) {
  float f[4], g[4]; acc_rd4n<A>(f); acc_rd4n<B>(g);
  u32x4 v; v[0] = cvtpk(f[0] * sc, f[1] * sc); v[1] = cvtpk(f[2] * sc, f[3] * sc); v[2] = cvtpk(g[0] * sc, g[1] * sc); v[3] = cvtpk(g[2] * sc, g[3] * sc);
  return v;
}
template <int A, int B> DI u32x4 acc_pack(float sc) {
  float f[4], g[4]; acc_rd4<A>(f); acc_rd4n<B>(g);
  u32x4 v; v[0] = cvtpk(f[0] * sc, f[1] * sc); v[1] = cvtpk(f[2] * sc, f[3] * sc); v[2] = cvtpk(g[0] * sc, g[1] * sc); v[3] = cvtpk(g[2] * sc, g[3] * sc);
  return v;
}
template <int A, int B> DI u32x4 acc_pack1() {
  float f[4], g[4]; acc_rd4<A>(f); acc_rd4n<B>(g);
  u32x4 v; v[0] = cvtpk(f[0], f[1]); v[1] = cvtpk(f[2], f[3]); v[2] = cvtpk(g[0], g[1]); v[3] = cvtpk(g[2], g[3]);
  return v;
}
template <int AUX> DI void st16(srd_t srd, unsigned voff, unsigned soff, u32x4 v) { __builtin_amdgcn_raw_buffer_store_b128(v, srd, voff, soff, AUX); }

constexpr int XSLOT = 32768, WBASE = 98304, WSLOT = 16384;
struct XStream { srd_t srd; unsigned v[8]; unsigned soff; };
struct WStream { srd_t srd; unsigned v[4]; unsigned soff, gstep; };
DI void issueX(XStream& X, unsigned ldsaddr, int half) {
  setm0(ldsaddr + half * 4096u); nop1();
  dma16o<0>(X.v[4 * half + 0], X.srd, X.soff); dma16o<1024>(X.v[4 * half + 1], X.srd, X.soff);
  dma16o<2048>(X.v[4 * half + 2], X.srd, X.soff); dma16o<3072>(X.v[4 * half + 3], X.srd, X.soff);
  if (half) X.soff += 128u;
}
DI void issueW(WStream& W, unsigned ldsaddr) {
  setm0(ldsaddr); nop1();
  dma16o<0>(W.v[0], W.srd, W.soff); dma16o<1024>(W.v[1], W.srd, W.soff); dma16o<2048>(W.v[2], W.srd, W.soff); dma16o<3072>(W.v[3], W.srd, W.soff);
  W.soff += W.gstep;
}
DI void makeW4(WStream& W, const void* p, unsigned bytes, unsigned ldw, unsigned col0, unsigned w, unsigned lane) {
  W.srd = mksrd(p, bytes);
#pragma unroll
  for (int j = 0; j < 4; ++j) W.v[j] = (2u * j + (lane >> 5)) * ldw + 16u * ((lane & 31u) ^ (((2u * (j & 1) + (lane >> 5)) << 2) | ((w & 1u) << 1))) - 1024u * j;
  W.soff = 8u * w * ldw + col0 * 2u; W.gstep = 32u * ldw;
}
struct Frags { bf16x8 x[8], w[8]; bf16x4 wl[8], wh[8];
  __device__ __forceinline__ void join() {
#pragma unroll
    for (int n = 0; n < 8; ++n) w[n] = __builtin_shufflevector(wl[n], wh[n], 0, 1, 2, 3, 4, 5, 6, 7);
  } };
template <int AD> DI void mfma16_a(const bf16x8& a, const bf16x8& b) {
  asm volatile("v_mfma_f32_16x16x32_bf16 a[%c2:%c3], %0, %1, a[%c2:%c3]" :: "v"(a), "v"(b), "i"(AD), "i"(AD + 3));
}
DI bf16x4 readWh(LAS char* lds, unsigned addr, int off) { return __builtin_amdgcn_ds_read_tr16_b64_v4bf16((LAS bf16x4*)(lds + addr + off)); }
DI bf16x8 readX(LAS char* lds, unsigned addr, int off) { return *(LAS bf16x8*)(lds + addr + off); }

struct Prefetch { srd_t s0; unsigned v0, j0, so, step; };
DI void pf_load(unsigned& j, unsigned voff, srd_t s) { asm volatile("s_nop 4\n\tbuffer_load_dword %0, %1, %2, 0 offen" : "=v"(j) : "v"(voff), "s"(s) : "memory"); }
DI void pf_slot(Prefetch& p) { asm volatile("buffer_load_dword %0, %1, %2, %3 offen" : "+v"(p.j0) : "v"(p.v0), "s"(p.s0), "s"(p.so) : "memory"); p.so += p.step; }
constexpr int kv_hook_ops(int e4) { return e4 == 1 ? 17 : (e4 == 2 ? 5 : (e4 == 3 ? 12 : 0)); }
template <bool VT, class Hook>
DI void kv_loop(Frags& f0, Frags& f1, LAS char* lds, unsigned ldsb, XStream& X, WStream& W, const unsigned (&xb)[2], const unsigned (&wb)[8], unsigned w, Prefetch& pf, Hook hook) {
  constexpr int S = 32;
  unsigned xs = 0;
  auto sub = [&](auto E4, auto LASTT, const Frags& cur, Frags& nxt) {
    constexpr int e4 = decltype(E4)::value, e = e4 & 1;
    constexpr bool LAST = decltype(LASTT)::value;
    constexpr int wo = e4 * WSLOT, won = ((e4 + 1) & 3) * WSLOT;
    unsigned xsn = xs; if (e == 1) xsn = (xs == 2 * XSLOT) ? 0u : xs + XSLOT;
    const unsigned xa = xb[(e + 1) & 1] + xsn;
    const unsigned xl = ldsb + ((e == 1) ? xs : ((xs == 0) ? 2 * XSLOT : xs - XSLOT)) + w * 8192u + ((e + 1) & 1) * 4096u;
    const unsigned wl = ldsb + WBASE + wo + w * 4096u;
    constexpr int hx = 4 * ((e + 1) & 1);
    sfor<64>([&](auto I) {
      constexpr int i = I, m = i / 8, n = i % 8;
      if constexpr (VT) mfma16_a<4 * (8 * m + n)>(cur.x[m], cur.w[n]); else mfma16_a<4 * (8 * m + n)>(cur.w[n], cur.x[m]);
      if constexpr (i == 1) {
        SB();
        if constexpr (!LAST) waitvl<(e == 0 ? 17 : 18)>(); else if constexpr (e4 == 0) waitvl<17>(); else if constexpr (e4 == 1) waitvl<0>(); else if constexpr (e4 == 2) waitvl<kv_hook_ops(1)>(); else waitvl<63>();
        BAR();
      }
      if constexpr (i >= 2 && !(LAST && e4 == 3)) {
        constexpr int k = i - 2;
        if constexpr (k == 0) nxt.wl[0] = readWh(lds, wb[0], won);
        else if constexpr (k == 1) nxt.wh[0] = readWh(lds, wb[0], won + 2048);
        else if constexpr (k == 2) nxt.x[0] = readX(lds, xa, 0);
        else if constexpr (k < 17) {
          constexpr int kk = k - 3, nn = 1 + kk / 2;
          if constexpr ((kk & 1) == 0) nxt.wl[nn] = readWh(lds, wb[nn], won); else nxt.wh[nn] = readWh(lds, wb[nn], won + 2048);
        } else if constexpr (k < 24) { constexpr int mm = 1 + k - 17; nxt.x[mm] = readX(lds, xa, 2048 * mm); }
      }
      if constexpr (!LAST) {
        constexpr int d0 = 26;
        if constexpr (i >= d0 && (i - d0) % 3 == 0 && (i - d0) / 3 < 10) {
          constexpr int d = (i - d0) / 3;
          if constexpr (d == 0) setm0(wl);
          else if constexpr (d == 1) dma16o<0>(W.v[0], W.srd, W.soff);
          else if constexpr (d == 2) dma16o<1024>(W.v[1], W.srd, W.soff);
          else if constexpr (d == 3) dma16o<2048>(W.v[2], W.srd, W.soff);
          else if constexpr (d == 4) dma16o<3072>(W.v[3], W.srd, W.soff);
          else if constexpr (d == 5) setm0(xl);
          else if constexpr (d == 6) dma16o<0>(X.v[hx + 0], X.srd, X.soff);
          else if constexpr (d == 7) dma16o<1024>(X.v[hx + 1], X.srd, X.soff);
          else if constexpr (d == 8) dma16o<2048>(X.v[hx + 2], X.srd, X.soff);
          else dma16o<3072>(X.v[hx + 3], X.srd, X.soff);
        }
        if constexpr (e == 0 && i == 57) pf_slot(pf);
      } else hook(E4, I);
      SB();
    });
    if constexpr (!LAST) { W.soff += W.gstep; if (((e + 1) & 1) == 1) X.soff += 128u; }
    xs = xsn;
    if constexpr (!(LAST && e4 == 3)) nxt.join();
  };
#pragma unroll 1
  for (int s = 0; s < S - 4; s += 4) {
    sub(IC<0>{}, std::false_type{}, f0, f1);
    sub(IC<1>{}, std::false_type{}, f1, f0);
    sub(IC<2>{}, std::false_type{}, f0, f1);
    if (s + 3 == S - 5) X.soff = 0x40000000u;
    sub(IC<3>{}, std::false_type{}, f1, f0);
  }
  sub(IC<0>{}, std::true_type{}, f0, f1);
  sub(IC<1>{}, std::true_type{}, f1, f0);
  sub(IC<2>{}, std::true_type{}, f0, f1);
  sub(IC<3>{}, std::true_type{}, f1, f0);
}

constexpr int AO_(int db, int qb) { return 8 * db + 4 * qb; }
constexpr int AQ_(int qb, int ds) { return 128 + 32 * qb + 4 * ds; }
constexpr int P1_WS = 24576, P1_XB = 98304, P1_XS = 20480, P1_G = 159744;
constexpr int p1_use(int f) { return f < 18 ? 2 * f : (f < 22 ? 36 + 3 * (f - 18) : 48); }
constexpr int P1_LA = 16, P1_R = 14, P1_NF = 14 * 23, P1_BAR = 21;
constexpr int p1_rd(int F) { return 50 * (F / 23) + p1_use(F % 23) - P1_LA; }
constexpr int p1_ni(int F) { return (F % 23) == 22 ? 1 : 2; }
constexpr int p1_reg(int F) { return 200 + 4 * (F % P1_R); }
constexpr int p1_xrd(int ks, int tb) { return 50 * (ks - 1) + 22 + 3 * tb; }
constexpr int p1_lds_between(int g0, bool after_w, int G) {
  int n = 0;
  for (int F = 0; F < P1_NF; ++F) for (int i = 0; i < p1_ni(F); ++i) { const int g = p1_rd(F) + i; if (g > g0 && g < G) ++n; }
  for (int ks = 1; ks < 14; ++ks) for (int tb = 0; tb < 3; ++tb) { const int g = p1_xrd(ks, tb); if ((g > g0 || (g == g0 && after_w)) && g < G) ++n; }
  return n;
}
constexpr bool p1_xiss(int ks) { return (ks & 1) == 1 && (ks + 5) / 2 < 7; }
#define KV_PRE 12
#define KV_LASTK 10
constexpr int p1_tot(int k) { return k < KV_LASTK ? ((32 - KV_PRE) * (k + 1) / KV_LASTK - (32 - KV_PRE) * k / KV_LASTK) : 0; }
constexpr int p1_nb(int k) { return p1_tot(k) > 0 ? 1 : 0; }
constexpr int p1_na(int k) { return p1_tot(k) - p1_nb(k); }
constexpr int p1_st0(int k) { int n = KV_PRE; for (int j = 0; j < k; ++j) n += p1_nb(j) + p1_na(j); return n; }
constexpr int p1_ops_before_barrier(int ks, int nst) {
  int n = 34 + (nst == 2 ? 32 : KV_PRE);
  for (int k = 0; k < ks; ++k) n += (nst == 1 ? 1 : 0) * (p1_nb(k) + p1_na(k)) + (k + 3 < 14 ? 6 : 0) + (p1_xiss(k) ? 5 : 0);
  return n + (nst == 1 ? 1 : 0) * p1_nb(ks);
}
constexpr int p1_wslot(int j) { return ((j + 2) % 4) * P1_WS; }
constexpr int p1_end_W(int j, int nst) {
  if (j == 0) return 11; if (j == 1) return 17; if (j == 2) return 29;
  return p1_ops_before_barrier(j - 3, nst) + 6;
}
constexpr int p1_end_x(int p, int nst) {
  if (p == 0) return 5; if (p == 1) return 22; if (p == 2) return 34;
  const int ks = 2 * p - 5;
  return p1_ops_before_barrier(ks, nst) + (ks + 3 < 14 ? 6 : 0) + 5;
}
constexpr int p1_wait(int ks, int nst) {
  int need = p1_end_W(ks + 1, nst);
  if (ks & 1) { const int e = p1_end_x((ks + 1) / 2, nst); if (e > need) need = e; }
  if (ks == 0 && need < 23) need = 23;
  return p1_ops_before_barrier(ks, nst) - need;
}
constexpr int ABq(int d, int qb) { return 128 + 8 * d + 4 * qb; }
constexpr int ABk(int d, int tb) { return 144 + 12 * d + 4 * tb; }
constexpr int ABv(int d, int tb) { return 168 + 12 * d + 4 * tb; }
constexpr int AZ(int qb) { return 192 + 4 * qb; }

template <int AD, int AF, bool Z> DI void mfK(const u32x4& x) {
  if constexpr (Z) asm volatile("v_mfma_f32_16x16x32_bf16 a[%c1:%c2], a[%c3:%c4], %0, 0" :: "v"(x), "i"(AD), "i"(AD + 3), "i"(AF), "i"(AF + 3));
  else asm volatile("v_mfma_f32_16x16x32_bf16 a[%c1:%c2], a[%c3:%c4], %0, a[%c1:%c2]" :: "v"(x), "i"(AD), "i"(AD + 3), "i"(AF), "i"(AF + 3));
}
template <int AD, int AF, bool Z> DI void mfV(const u32x4& x) {
  if constexpr (Z) asm volatile("v_mfma_f32_16x16x32_bf16 a[%c1:%c2], %0, a[%c3:%c4], 0" :: "v"(x), "i"(AD), "i"(AD + 3), "i"(AF), "i"(AF + 3));
  else asm volatile("v_mfma_f32_16x16x32_bf16 a[%c1:%c2], %0, a[%c3:%c4], a[%c1:%c2]" :: "v"(x), "i"(AD), "i"(AD + 3), "i"(AF), "i"(AF + 3));
}
template <int A, int OFF> DI void tr_rd(unsigned base) { asm volatile("ds_read_b64_tr_b16 a[%c1:%c2], %0 offset:%c3" :: "v"(base), "i"(A), "i"(A + 1), "i"(OFF) : "memory"); }
template <int A, int OFF> DI void frag_rd(unsigned base) { asm volatile("ds_read_b128 a[%c1:%c2], %0 offset:%c3" :: "v"(base), "i"(A), "i"(A + 3), "i"(OFF) : "memory"); }
template <int OFF> DI void lds_rdv(u32x4& r, unsigned base) { asm volatile("ds_read_b128 %0, %1 offset:%c2" : "=v"(r) : "v"(base), "i"(OFF) : "memory"); }
DI void mfma_vv0(f32x4& d, const u32x4& a, const u32x4& b) { asm volatile("v_mfma_f32_16x16x32_bf16 %0, %1, %2, 0" : "=&v"(d) : "v"(a), "v"(b)); }
DI void mfma_vvc(f32x4& d, const u32x4& a, const u32x4& b, const f32x4& c) { asm volatile("v_mfma_f32_16x16x32_bf16 %0, %1, %2, %3" : "=&v"(d) : "v"(a), "v"(b), "v"(c)); }
DI float xmax16_32(float v) {
  auto a = __builtin_amdgcn_permlane32_swap(__float_as_uint(v), __float_as_uint(v), false, false);
  v = fmaxf(__uint_as_float(a[0]), __uint_as_float(a[1]));
  auto b = __builtin_amdgcn_permlane16_swap(__float_as_uint(v), __float_as_uint(v), false, false);
  return fmaxf(__uint_as_float(b[0]), __uint_as_float(b[1]));
}
DI float xsum16_32(float v) {
  auto a = __builtin_amdgcn_permlane32_swap(__float_as_uint(v), __float_as_uint(v), false, false);
  v = __uint_as_float(a[0]) + __uint_as_float(a[1]);
  auto b = __builtin_amdgcn_permlane16_swap(__float_as_uint(v), __float_as_uint(v), false, false);
  return __uint_as_float(b[0]) + __uint_as_float(b[1]);
}
template <int A> DI void acc_wr4(const u32x4& v) {
  asm volatile("v_accvgpr_write_b32 a%c4, %0\n\tv_accvgpr_write_b32 a%c5, %1\n\tv_accvgpr_write_b32 a%c6, %2\n\tv_accvgpr_write_b32 a%c7, %3"
               :: "v"(v[0]), "v"(v[1]), "v"(v[2]), "v"(v[3]), "i"(A), "i"(A + 1), "i"(A + 2), "i"(A + 3) : ALL_AGPRS);
}

struct P1 {
  WStream W; srd_t sB, sX;
  unsigned vB[2], vX[5], sBo, sXo;
};
DI void p1_setup(P1& c, const bf16_t* x, const bf16_t* wq, const bf16_t* wqkv, int head, int T, unsigned w, unsigned lane) {
  makeW4(c.W, wq, DM * 4096 * 2, 8192, head * 256, w, lane);
  c.sB = mksrd(wqkv, DM * 1344 * 2);
#pragma unroll
  for (int jj = 0; jj < 2; ++jj) {
    const unsigned ch = (lane & 15u) ^ ((((lane >> 4) & 3u) << 2) | ((w & 1u) << 1)), sec = ch >> 2;
    c.vB[jj] = (sec == 3u) ? 0x7fffff00u : ((4u * jj + (lane >> 4)) * 2688u + sec * 896u + 56u * head + 16u * (ch & 3u) - 1024u * jj);
  }
  c.sBo = 8u * w * 2688u;
  c.sX = mksrd((const char*)x - 4096, SEQ * DM * 2 + 4096);
#pragma unroll
  for (int j = 0; j < 5; ++j) {
    const int row = 40 * (int)w + 8 * j + (int)(lane >> 3), tok = 128 * T - 16 + row;
    const unsigned sw = (4u * (w + j) + (lane >> 4)) & 7u;
    c.vX[j] = (tok < 0 || tok >= SEQ) ? 0x7fffff00u : (4096u + (unsigned)tok * 896u + 16u * ((lane & 7u) ^ sw) - 1024u * (j & 3));
  }
  c.sXo = 0u;
}
template <int STEP> DI void p1_W_step(P1& c, unsigned ldsb, int slot_off, unsigned w) {
  if constexpr (STEP == 0) setm0(ldsb + slot_off + w * 4096u);
  else if constexpr (STEP <= 4) dma16o<1024 * (STEP - 1)>(c.W.v[STEP - 1], c.W.srd, c.W.soff);
  else if constexpr (STEP == 5) setm0(ldsb + slot_off + 16384u + w * 2048u);
  else if constexpr (STEP == 6) dma16o<0>(c.vB[0], c.sB, c.sBo);
  else { dma16o<1024>(c.vB[1], c.sB, c.sBo); c.W.soff += c.W.gstep; c.sBo += 32u * 2688u; }
}
template <int STEP> DI void p1_x_step(P1& c, unsigned ldsb, int slot_off, unsigned w) {
  if constexpr (STEP == 0) setm0(ldsb + slot_off + w * 5120u);
  else if constexpr (STEP <= 4) dma16o<1024 * (STEP - 1)>(c.vX[STEP - 1], c.sX, c.sXo);
  else if constexpr (STEP == 5) setm0(ldsb + slot_off + w * 5120u + 4096u);
  else { dma16o<0>(c.vX[4], c.sX, c.sXo); c.sXo += 128u; }
}
template <int E4, int I> DI void p1_preamble(P1& c, srd_t sg, unsigned ldsb, unsigned w, unsigned lane) {
  if constexpr (I >= 4 && (I & 1) == 0) {
    constexpr int k = (I - 4) / 2;
    if constexpr (E4 == 1) {
      if constexpr (k < 7) p1_x_step<k>(c, ldsb, P1_XB + 0 * P1_XS, w);
      else if constexpr (k < 15) p1_W_step<k - 7>(c, ldsb, p1_wslot(0), w);
      else if constexpr (k < 23) p1_W_step<k - 15>(c, ldsb, p1_wslot(1), w);
    } else if constexpr (E4 == 2) {
      if constexpr (k < 7) p1_x_step<k>(c, ldsb, P1_XB + 1 * P1_XS, w);
    } else if constexpr (E4 == 3) {
      if constexpr (k == 0) setm0(ldsb + P1_G + w * 1024u);
      else if constexpr (k == 1) dma16o<0>(lane * 16u, sg, 0u);
      else if constexpr (k < 10) p1_W_step<k - 2>(c, ldsb, p1_wslot(2), w);
      else if constexpr (k < 17) p1_x_step<k - 10>(c, ldsb, P1_XB + 2 * P1_XS, w);
    }
  }
}

struct P1Out { float g[2]; u32x4 ob[2]; };

template <int NST, class St, class Done, class Early>
DI void p1_loop(P1& c, unsigned ldsb, unsigned w, unsigned lane, St st, Done done, Early early ST_ARG) {
  const unsigned r16 = lane & 15u, g = lane >> 4, q = (lane >> 2) & 3u, p = lane & 3u;
  unsigned bq[2][8], bb[2][6], bx[2];
  const unsigned fz = (q << 1) | (g & 1u);
#pragma unroll
  for (int s = 0; s < 2; ++s) {
#pragma unroll
    for (int n = 0; n < 8; ++n) bq[s][n] = ldsb + s * 2 * P1_WS + 512u * (8u * g + q) + 32u * ((unsigned)n ^ fz) + 8u * p;
#pragma unroll
    for (int n = 0; n < 6; ++n) bb[s][n] = ldsb + s * 2 * P1_WS + 16384u + 256u * (8u * g + q) + 32u * ((unsigned)n ^ fz) + 8u * p;
    bx[s] = ldsb + P1_XB + 128u * (32u * w + r16) + 16u * ((4u * s + g) ^ ((r16 >> 1) & 7u));
  }
  const unsigned bg = ldsb + P1_G + w * 1024u + 16u * g;
  u32x4 xa[2][3];
  auto rd_instr = [&](auto FF, auto II) {
    constexpr int F = FF, i = II, ks = F / 23, f = F % 23, sl = (ks + 2) % 4, set = sl / 2, so = (sl % 2) * P1_WS, reg = p1_reg(F);
    if constexpr (f < 16) tr_rd<reg + 2 * i, so + 256 * (f >> 3) + 2048 * i>(bq[set][f & 7]);
    else if constexpr (f < 22) tr_rd<reg + 2 * i, so + 1024 * i>(bb[set][f - 16]);
    else frag_rd<reg, 64 * ks>(bg);
  };
  auto rd_x = [&](auto KS, auto TB) { constexpr int ks = KS, tb = TB; lds_rdv<((ks >> 1) % 3) * P1_XS + 2048 * tb>(xa[ks & 1][tb], bx[ks & 1]); };
  waitv<34 - 11 + (NST == 2 ? 32 : KV_PRE)>();
  BAR();
  sfor<3>([&](auto TB) { rd_x(IC<0>{}, TB); });
  sfor<8>([&](auto FF) { rd_instr(FF, IC<0>{}); rd_instr(FF, IC<1>{}); });
  waitl<0>();
  pin(xa[0][0], xa[0][1], xa[0][2]);
  SB();
  sfor<14>([&](auto KS) {
    constexpr int ks = KS;
    STAMP(16 + ks);
    sfor<50>([&](auto GG) {
      constexpr int gg = GG, G = 50 * ks + gg;
      constexpr int f = gg < 36 ? gg / 2 : (gg < 48 ? 18 + (gg - 36) / 3 : 22), sub = gg - p1_use(f), F = 23 * ks + f, reg = p1_reg(F);
      constexpr bool Z = ks == 0;
      if constexpr (gg == 0 && ks > 0) {
        constexpr int n = p1_lds_between(p1_xrd(ks, 2), false, G);
        waitl<(n > 15 ? 15 : n)>();
        pin(xa[ks & 1][0], xa[ks & 1][1], xa[ks & 1][2]);
      }
      if constexpr (sub == 0 && (F % 2) == 0 && p1_rd(F) >= 0) {
        constexpr int FN = (F + 1 < P1_NF) ? F + 1 : F;
        constexpr int n = p1_lds_between(p1_rd(FN) + p1_ni(FN) - 1, true, G);
        static_assert(p1_rd(FN) + p1_ni(FN) <= G, "read too late");
        waitl<(n > 15 ? 15 : n)>();
      }
      if constexpr (f < 16) mfK<AO_(f, sub), reg, Z>(xa[ks & 1][1 + sub]);
      else if constexpr (f < 18) mfK<ABq(f - 16, sub), reg, Z>(xa[ks & 1][1 + sub]);
      else if constexpr (f < 20) mfK<ABk(f - 18, sub), reg, Z>(xa[ks & 1][sub]);
      else if constexpr (f < 22) mfV<ABv(f - 20, sub), reg, Z>(xa[ks & 1][sub]);
      else mfK<AZ(sub), reg, Z>(xa[ks & 1][1 + sub]);
      if constexpr (gg == P1_BAR && ks < 13) {
        constexpr int N = p1_wait(ks, NST);
        static_assert(N >= 0 && N < 63, "vmcnt range");
        if constexpr ((NST == 1 && ks == KV_LASTK + 1) || (NST == 2 && ks == KV_DONE_KS)) { waitv<0>(); BAR(); done(); }
        else { waitv<N>(); BAR(); }
      }
      sfor<12>([&](auto DD) {
        constexpr int H = 23 * ks + f + (int)DD;
        if constexpr (H < P1_NF) {
          if constexpr (p1_rd(H) == G) rd_instr(IC<H>{}, IC<0>{});
          else if constexpr (p1_ni(H) == 2 && p1_rd(H) + 1 == G) rd_instr(IC<H>{}, IC<1>{});
        }
      });
      if constexpr (ks < 13) { sfor<3>([&](auto TB) { if constexpr (p1_xrd(ks + 1, TB) == G) rd_x(IC<ks + 1>{}, TB); }); }
      if constexpr (ks + 3 < 14) {
        constexpr int so = p1_wslot(ks + 3);
        if constexpr (gg == 23) setm0(ldsb + so + w * 4096u);
        if constexpr (gg == 24) dma16o<0>(c.W.v[0], c.W.srd, c.W.soff);
        if constexpr (gg == 26) dma16o<1024>(c.W.v[1], c.W.srd, c.W.soff);
        if constexpr (gg == 27) dma16o<2048>(c.W.v[2], c.W.srd, c.W.soff);
        if constexpr (gg == 29) dma16o<3072>(c.W.v[3], c.W.srd, c.W.soff);
        if constexpr (gg == 30) setm0(ldsb + so + 16384u + w * 2048u);
        if constexpr (gg == 31) dma16o<0>(c.vB[0], c.sB, c.sBo);
        if constexpr (gg == 33) dma16o<1024>(c.vB[1], c.sB, c.sBo);
        if constexpr (gg == 34) { c.W.soff += c.W.gstep; c.sBo += 32u * 2688u; }
      }
      if constexpr (p1_xiss(ks)) {
        constexpr int so = P1_XB + (((ks + 5) / 2) % 3) * P1_XS;
        if constexpr (gg == 35) setm0(ldsb + so + w * 5120u);
        if constexpr (gg == 36) dma16o<0>(c.vX[0], c.sX, c.sXo);
        if constexpr (gg == 38) dma16o<1024>(c.vX[1], c.sX, c.sXo);
        if constexpr (gg == 39) dma16o<2048>(c.vX[2], c.sX, c.sXo);
        if constexpr (gg == 41) dma16o<3072>(c.vX[3], c.sX, c.sXo);
        if constexpr (gg == 42) setm0(ldsb + so + w * 5120u + 4096u);
        if constexpr (gg == 43) dma16o<0>(c.vX[4], c.sX, c.sXo);
        if constexpr (gg == 44) c.sXo += 128u;
      }
      if constexpr (NST == 1) {
        constexpr int b0 = p1_st0(ks), nb = p1_nb(ks), na = p1_na(ks);
        if constexpr (gg == 4 && nb >= 1) st(IC<(b0 < 32 ? b0 : 0)>{});
        if constexpr (gg == 14 && nb >= 2) st(IC<(b0 + 1 < 32 ? b0 + 1 : 0)>{});
        if constexpr (gg == 38 && na >= 3) st(IC<(b0 + nb < 32 ? b0 + nb : 0)>{});
        if constexpr (gg == 43 && na >= 2) st(IC<(b0 + nb + (na >= 3 ? 1 : 0) < 32 ? b0 + nb + (na >= 3 ? 1 : 0) : 0)>{});
        if constexpr (gg == 48 && na >= 1) st(IC<(b0 + nb + na - 1 < 32 && na >= 1 ? b0 + nb + na - 1 : 0)>{});
      }
      if constexpr (ks == 13 && gg == 24) early();
      SB();
    });
  });
  STAMP(30);
}

struct P1B { u32x4 kf[3], vf[2][2], qf[2]; float omg[2]; };
DI void p1_post_b1(P1B& B, P1Out& o, unsigned lane) {
  const unsigned g = lane >> 4;
  float (&omg)[2] = B.omg;
#pragma unroll
  for (int qb = 0; qb < 2; ++qb) {
    float f[4];
    if (qb == 0) acc_rd4n<AZ(0)>(f); else acc_rd4n<AZ(1)>(f);
    const float e = __builtin_amdgcn_exp2f(-LOG2E * f[0]);
    o.g[qb] = __builtin_amdgcn_rcpf(1.f + e);
    omg[qb] = 1.f - o.g[qb];
  }
  {
    u32x4 (&kf)[3] = B.kf; u32x4 (&vf)[2][2] = B.vf; u32x4 (&qf)[2] = B.qf;
    sfor<3>([&](auto TB) { kf[TB] = acc_pack1n<ABk(0, TB), ABk(1, TB)>(); });
    sfor<2>([&](auto QB) {
      qf[QB] = acc_packn<ABq(0, QB), ABq(1, QB)>(BSCALE);
      if (g == 3u) { qf[QB][2] = 0u; qf[QB][3] = 0u; }
      sfor<2>([&](auto D) { vf[QB][D] = acc_pack1n<ABv(D, QB), ABv(D, QB + 1)>(); });
    });
  }
}
DI void p1_post_b2(P1B& B, P1Out& o, int T, unsigned w, unsigned lane) {
  const unsigned r16 = lane & 15u, g = lane >> 4;
  const int T0 = 128 * T + 32 * (int)w;
  {
    u32x4 (&kf)[3] = B.kf; u32x4 (&vf)[2][2] = B.vf; u32x4 (&qf)[2] = B.qf; float (&omg)[2] = B.omg;
    u32x4 ones = {0x3f803f80u, 0x3f803f80u, 0x3f803f80u, 0x3f803f80u};
    f32x4 cin[2][2], s[2][2];
#pragma unroll
    for (int qb = 0; qb < 2; ++qb)
#pragma unroll
      for (int kk = 0; kk < 2; ++kk)
#pragma unroll
        for (int r = 0; r < 4; ++r) {
          const int diff = (int)r16 - 4 * (int)g - r + 16 * (1 - kk);
          const int key = T0 + 16 * qb + (int)r16 - diff;
          cin[qb][kk][r] = (diff >= 0 && diff <= 14 && (diff & 1) == 0 && key >= 0) ? 0.f : -__builtin_inff();
        }
    pin(cin[0][0], cin[0][1], cin[1][0], cin[1][1], qf[0], qf[1], kf[0], kf[1], kf[2], ones); snop<1>();
    mfma_vvc(s[0][0], kf[0], qf[0], cin[0][0]); mfma_vvc(s[0][1], kf[1], qf[0], cin[0][1]);
    mfma_vvc(s[1][0], kf[1], qf[1], cin[1][0]); mfma_vvc(s[1][1], kf[2], qf[1], cin[1][1]);
    snop<9>(); pin(s[0][0], s[0][1], s[1][0], s[1][1]);
    u32x4 pf[2];
#pragma unroll
    for (int qb = 0; qb < 2; ++qb) {
      float m = fmaxf(fmaxf(fmaxf(s[qb][0][0], s[qb][0][1]), fmaxf(s[qb][0][2], s[qb][0][3])), fmaxf(fmaxf(s[qb][1][0], s[qb][1][1]), fmaxf(s[qb][1][2], s[qb][1][3])));
      m = xmax16_32(m);
      pf[qb][0] = cvtpk(__builtin_amdgcn_exp2f(s[qb][0][0] - m), __builtin_amdgcn_exp2f(s[qb][0][1] - m)); pf[qb][1] = cvtpk(__builtin_amdgcn_exp2f(s[qb][0][2] - m), __builtin_amdgcn_exp2f(s[qb][0][3] - m));
      pf[qb][2] = cvtpk(__builtin_amdgcn_exp2f(s[qb][1][0] - m), __builtin_amdgcn_exp2f(s[qb][1][1] - m)); pf[qb][3] = cvtpk(__builtin_amdgcn_exp2f(s[qb][1][2] - m), __builtin_amdgcn_exp2f(s[qb][1][3] - m));
    }
    f32x4 o0[2], o1[2], ls[2];
    pin(pf[0], pf[1], vf[0][0], vf[0][1], vf[1][0], vf[1][1]); snop<1>();
    mfma_vv0(o0[0], vf[0][0], pf[0]); mfma_vv0(o1[0], vf[0][1], pf[0]); mfma_vv0(ls[0], ones, pf[0]);
    mfma_vv0(o0[1], vf[1][0], pf[1]); mfma_vv0(o1[1], vf[1][1], pf[1]); mfma_vv0(ls[1], ones, pf[1]);
    snop<9>(); pin(o0[0], o1[0], ls[0], o0[1], o1[1], ls[1]);
#pragma unroll
    for (int qb = 0; qb < 2; ++qb) {
      const float sc = omg[qb] * __builtin_amdgcn_rcpf(ls[qb][0]);
      u32x4 ob; ob[0] = cvtpk(o0[qb][0] * sc, o0[qb][1] * sc); ob[1] = cvtpk(o0[qb][2] * sc, o0[qb][3] * sc); ob[2] = cvtpk(o1[qb][0] * sc, o1[qb][1] * sc); ob[3] = cvtpk(o1[qb][2] * sc, o1[qb][3] * sc);
      if (g == 3u) { ob[2] = 0u; ob[3] = 0u; }
      o.ob[qb] = ob;
    }
  }
}
template <class QSt> DI void p1_post_q(QSt qst) {
  sfor<16>([&](auto I) {
    constexpr int qb = I >> 3, ds = I & 7;
    const u32x4 v = acc_pack<AO_(2 * ds, qb), AO_(2 * ds + 1, qb)>(QSCALE);
    qst(I, v);
    SB();
  });
}
constexpr float THR = 8.f;
constexpr int AF_(int seq) { return 192 + 4 * (seq & 15); }

struct Att {
  f32x4 S[2][4][2];
  f32x4 CIN[4][2];
  u32x4 P[2][2];
  f32x4 LT[2];
  float l[2];
  u32x4 ones;
  float mref[2], fct[2], mx, mx2;
  unsigned kb[2], vb[2];
  unsigned vlane;
  srd_t srdK, srdV;
  bool pend;
};
template <int AF, int AQ> DI void qk_first(f32x4& s, const f32x4& c) {
  asm volatile("v_mfma_f32_16x16x32_bf16 %0, a[%c2:%c3], a[%c4:%c5], %1" : "=&v"(s) : "v"(c), "i"(AF), "i"(AF + 3), "i"(AQ), "i"(AQ + 3));
}
template <int AF, int AQ> DI void qk_next(f32x4& s) {
  asm volatile("v_mfma_f32_16x16x32_bf16 %0, a[%c1:%c2], a[%c3:%c4], %0" : "+v"(s) : "i"(AF), "i"(AF + 3), "i"(AQ), "i"(AQ + 3));
}
template <int AD, int AF> DI void pv_mfma(const u32x4& p) {
  asm volatile("v_mfma_f32_16x16x32_bf16 a[%c1:%c2], a[%c3:%c4], %0, a[%c1:%c2]" :: "v"(p), "i"(AD), "i"(AD + 3), "i"(AF), "i"(AF + 3));
}
DI void l_mfma0(f32x4& l, const u32x4& ones, const u32x4& p) {
  asm volatile("v_mfma_f32_16x16x32_bf16 %0, %1, %2, 0" : "=&v"(l) : "v"(ones), "v"(p));
}
DI void l_mfma1(f32x4& l, const u32x4& ones, const u32x4& p) {
  asm volatile("v_mfma_f32_16x16x32_bf16 %0, %1, %2, %0" : "+v"(l) : "v"(ones), "v"(p));
}
DI float vexp(float x) { float r = __builtin_amdgcn_exp2f(x); asm volatile("" : "+v"(r)); return r; }
DI float vmax3(float a, float b, float c) { float r = __builtin_elementwise_maximum(__builtin_elementwise_maximum(a, b), c); asm volatile("" : "+v"(r)); return r; }
DI unsigned vcvt(float lo, float hi) { unsigned r = cvtpk(lo, hi); asm volatile("" : "+v"(r)); return r; }

constexpr int NPRE = 8;
constexpr int rdgap(int seq) { return (seq < 32 ? 2 * seq : 68 + 2 * (seq - 32)) - 16; }
constexpr int usegap(int seq) { return seq < 32 ? 2 * seq : 68 + 2 * (seq - 32); }
constexpr int nafter(int seq, int g) {
  int n = 0;
  for (int s = 0; s < 64; ++s) for (int p = -1; p <= 1; ++p) { const int r = rdgap(s) + 132 * p; if (r > rdgap(seq) && r < g) ++n; }
  return n;
}
constexpr int WG = 4;
template <int G> DI void maybe_wait() {
  constexpr int seq = (G < 64) ? ((G & 1) == 0 ? G / 2 : -1) : (G >= 68 && (G & 1) == 0 ? 32 + (G - 68) / 2 : -1);
  if constexpr (seq >= 0 && (seq % WG) == 0) {
    constexpr int n = nafter(seq + WG - 1, G);
    static_assert(rdgap(seq + WG - 1) < G, "fragment not yet requested");
    waitl<(n > 15 ? 15 : n)>();
  }
}
template <int G, int X> DI void do_reads(Att& c) {
  sfor<64>([&](auto S_) {
    constexpr int s = S_;
    if constexpr (rdgap(s) == G) {
      if constexpr (s < 32) frag_rd<AF_(s), s * 1024>(c.kb[X]); else frag_rd<AF_(s), (s - 32) * 1024>(c.vb[X ^ 1]);
    } else if constexpr (rdgap(s) + 132 == G) frag_rd<AF_(s), s * 1024>(c.kb[X ^ 1]);
  });
}

template <int X, int H, int E> DI void do_exp(Att& c) {
  constexpr int kb = 2 * H + (E >> 3), qb = (E >> 2) & 1, r = E & 3; c.S[X][kb][qb][r] = vexp(c.S[X][kb][qb][r]);
}
template <int X, int KS, int PP> DI void do_pack(Att& c) {
  constexpr int kbl = PP >> 2, qb = (PP >> 1) & 1, h = PP & 1, kb = 2 * KS + kbl;
  c.P[qb][KS][2 * kbl + h] = vcvt(c.S[X][kb][qb][2 * h], c.S[X][kb][qb][2 * h + 1]);
}
template <int X, int I> DI void do_max(Att& c) {
  constexpr int ch = I & 1, st = I >> 1, base = 16 * ch;
  auto SV = [&](auto J) -> float { constexpr int j = J; return c.S[X][j >> 3][(j >> 2) & 1][j & 3]; };
  float& m = ch ? c.mx2 : c.mx;
  if constexpr (st == 0) m = vmax3(SV(std::integral_constant<int, base>{}), SV(std::integral_constant<int, base + 1>{}), SV(std::integral_constant<int, base + 2>{}));
  else if constexpr (st < 7) m = vmax3(m, SV(std::integral_constant<int, base + 2 * st + 1>{}), SV(std::integral_constant<int, base + 2 * st + 2>{}));
  else if constexpr (ch == 0) m = vmax3(m, SV(std::integral_constant<int, base + 15>{}), SV(std::integral_constant<int, base + 15>{}));
  else m = vmax3(m, SV(std::integral_constant<int, base + 15>{}), c.mx);
}
template <int X, bool FIRSTT> DI void raise_max(Att& c) {
  asm volatile("s_nop 7" ::: "memory");
#pragma unroll
  for (int qb = 0; qb < 2; ++qb) {
    float m = c.S[X][0][qb][0];
#pragma unroll
    for (int kb = 0; kb < 4; ++kb)
#pragma unroll
      for (int r = 0; r < 4; ++r) m = fmaxf(m, c.S[X][kb][qb][r]);
    m = xmax16_32(m);
    float dl = FIRSTT ? fmaxf(m, -1e30f) : fmaxf(m, 0.f);
    c.mref[qb] += dl;
    c.fct[qb] = FIRSTT ? 1.f : c.fct[qb] * __builtin_amdgcn_exp2f(-dl);
#pragma unroll
    for (int kb = 0; kb < 4; ++kb)
#pragma unroll
      for (int r = 0; r < 4; ++r) c.S[X][kb][qb][r] -= dl;
  }
  if (!FIRSTT) c.pend = true;
}
DI void build_cin(Att& c, bool diag, int dq) {
  if (diag) {
#pragma unroll
    for (int kb = 0; kb < 4; ++kb)
#pragma unroll
      for (int qb = 0; qb < 2; ++qb)
#pragma unroll
        for (int r = 0; r < 4; ++r) c.CIN[kb][qb][r] = (16 * kb + r - 16 * qb > dq) ? -__builtin_inff() : -c.mref[qb];
  } else {
#pragma unroll
    for (int kb = 0; kb < 4; ++kb)
#pragma unroll
      for (int qb = 0; qb < 2; ++qb)
#pragma unroll
        for (int r = 0; r < 4; ++r) c.CIN[kb][qb][r] = -c.mref[qb];
  }
  asm volatile("s_nop 1" ::: "memory");
}
template <int A> DI void acc_scale(float f) {
  float t; asm volatile("v_accvgpr_read_b32 %0, a%c2\n\tv_mul_f32 %0, %0, %1\n\tv_accvgpr_write_b32 a%c2, %0" : "=&v"(t) : "v"(f), "i"(A) : ALL_AGPRS);
}
template <int A, int OFF, int AUX> DI void ld_acc16(unsigned voff, srd_t srd, unsigned soff) {
  asm volatile("s_nop 4\n\tbuffer_load_dwordx4 a[%c3:%c4], %0, %1, %2 offen offset:%c5 sc1" :: "v"(voff), "s"(srd), "s"(soff), "i"(A), "i"(A + 3), "i"(OFF) : "memory");
}
template <int A, int OFF> DI void ld_acc16n(unsigned voff, srd_t srd, unsigned soff) {
  asm volatile("buffer_load_dwordx4 a[%c3:%c4], %0, %1, %2 offen offset:%c5 sc1" :: "v"(voff), "s"(srd), "s"(soff), "i"(A), "i"(A + 3), "i"(OFF) : "memory");
}
template <int OFF> DI void ld_f32c(float& r, unsigned voff, srd_t srd) { asm volatile("buffer_load_dword %0, %1, %2, 0 offen offset:%c3 sc1" : "=v"(r) : "v"(voff), "s"(srd), "i"(OFF) : "memory"); }
DI void rescale_O(Att& c) {
  asm volatile("s_nop 7" ::: "memory");
  sfor<128>([&](auto I) { constexpr int a = I; acc_scale<a>(c.fct[(a >> 2) & 1]); });
  c.l[0] *= c.fct[0]; c.l[1] *= c.fct[1];
  asm volatile("s_nop 1" ::: "memory");
  c.fct[0] = 1.f; c.fct[1] = 1.f; c.pend = false;
}
template <int G8> DI void dmaK(Att& c, unsigned soff) { dma16c<1024 * (G8 & 3)>(c.vlane, c.srdK, (G8 >> 2) ? soff + 4096u : soff); }
template <int G8> DI void dmaKs(Att& c, unsigned soff) { dma16cs<1024 * (G8 & 3)>(c.vlane, c.srdK, (G8 >> 2) ? soff + 4096u : soff); }
template <int G8> DI void dmaVs(Att& c, unsigned soff) { dma16cs<1024 * (G8 & 3)>(c.vlane, c.srdV, (G8 >> 2) ? soff + 4096u : soff); }
template <int G8> DI void dmaV(Att& c, unsigned soff) { dma16c<1024 * (G8 & 3)>(c.vlane, c.srdV, (G8 >> 2) ? soff + 4096u : soff); }

template <int X, bool FIRST, class Mid> DI void phaseA(Att& c, Mid mid) {
  sfor<64>([&](auto G) {
    constexpr int g = G, k = g >> 1, qb = g & 1, kb = k >> 3, ds = k & 7;
    maybe_wait<g>();
    if constexpr (ds == 0) qk_first<AF_(k), AQ_(qb, 0)>(c.S[X][kb][qb], c.CIN[kb][qb]);
    else qk_next<AF_(k), AQ_(qb, ds)>(c.S[X][kb][qb]);
    if constexpr (g == 50) { waitvl<0>(); BAR(); mid(); }
    if constexpr (!FIRST || g < 50) do_reads<g, X>(c);
    if constexpr (!FIRST) {
      if constexpr ((g & 1) == 1 && g <= 31) do_exp<X ^ 1, 1, (g >> 1)>(c);
      if constexpr ((g & 1) == 0 && g <= 14) do_pack<X ^ 1, 0, (g >> 1)>(c);
      if constexpr ((g & 1) == 0 && g >= 34 && g <= 48) do_pack<X ^ 1, 1, ((g - 34) >> 1)>(c);
    }
    SB();
  });
}
template <int X, bool FIRST> DI void phaseB(Att& c, unsigned m0k, unsigned ksoff, unsigned m0v, unsigned vsoff) {
  sfor<68>([&](auto G) {
    constexpr int g = G;
    if constexpr (!FIRST) {
      maybe_wait<64 + g>();
      if constexpr (g < 2) l_mfma0(c.LT[g & 1], c.ones, c.P[g & 1][0]);
      else if constexpr (g < 4) l_mfma1(c.LT[g & 1], c.ones, c.P[g & 1][1]);
      else { constexpr int v = (g - 4) >> 1, qb = g & 1, db = v >> 1, ks = v & 1; pv_mfma<AO_(db, qb), AF_(32 + v)>(c.P[qb][ks]); }
    }
    if constexpr (FIRST && g == 0) asm volatile("s_nop 7" ::: "memory");
    if constexpr (!FIRST || 64 + g >= rdgap(0) + 132) do_reads<64 + g, X>(c);
    if constexpr (!FIRST) {
      if constexpr (g == 40) { pin(c.LT[0]); c.l[0] += c.LT[0][0]; pin(c.l[0]); }
      if constexpr (g == 42) { pin(c.LT[1]); c.l[1] += c.LT[1][0]; pin(c.l[1]); }
      if constexpr ((g & 1) == 1 && g <= 31) do_max<X, (g >> 1)>(c);
    }
    if constexpr (g == 0) setm0(m0v);
    if constexpr (g == 1 || g == 3 || g == 21 || g == 23) { constexpr int j = g == 1 ? 0 : (g == 3 ? 1 : (g == 21 ? 2 : 3)); if constexpr (FIRST) dmaVs<j>(c, vsoff); else dmaV<j>(c, vsoff); }
    if constexpr (g == 23) setm0(m0v + 4096u);
    if constexpr (g == 25 || g == 27 || g == 29 || g == 31) { if constexpr (FIRST) dmaVs<4 + ((g - 25) >> 1)>(c, vsoff); else dmaV<4 + ((g - 25) >> 1)>(c, vsoff); }
    if constexpr (g == 33) setm0(m0k);
    if constexpr (g == 34 || g == 36 || g == 40 || g == 42) { constexpr int j = g == 34 ? 0 : (g == 36 ? 1 : (g == 40 ? 2 : 3)); if constexpr (FIRST) dmaKs<j>(c, ksoff); else dmaK<j>(c, ksoff); }
    if constexpr (g == 42) setm0(m0k + 4096u);
    if constexpr (g == 44 || g == 46 || g == 48 || g == 50) { if constexpr (FIRST) dmaKs<4 + ((g - 44) >> 1)>(c, ksoff); else dmaK<4 + ((g - 44) >> 1)>(c, ksoff); }
    if constexpr (g == 32) {
      if constexpr (FIRST) raise_max<X, true>(c);
      else { if (__builtin_expect(__any(c.mx2 > THR), 0)) raise_max<X, false>(c); }
    }
    if constexpr ((g & 1) == 1 && g >= 35 && g <= 65) do_exp<X, 0, ((g - 35) >> 1)>(c);
    SB();
  });
}
template <int X, int KIND, class Start, class Pre, class Step, class Fin> DI void drain(Att& c, Start start, Pre pre, Step step, Fin fin) {
  start(IC<KIND>{});
  sfor<16>([&](auto I) { do_exp<X, 1, I>(c); });
  asm volatile("s_nop 0");
  sfor<8>([&](auto I) { do_pack<X, 0, I>(c); });
  sfor<8>([&](auto I) { do_pack<X, 1, I>(c); });
  waitvl<0>();
  BAR();
  pre(IC<KIND>{});
  sfor<12>([&](auto I) { frag_rd<AF_(32 + I), I * 1024>(c.vb[X]); });
  SB();
  sfor<68>([&](auto G) {
    constexpr int g = G;
    if constexpr (g >= 4 && (g & 1) == 0) { constexpr int v = (g - 4) >> 1; waitl<(v + 13 < 32 ? 13 : (31 - v))>(); }
    if constexpr (g < 2) l_mfma0(c.LT[g & 1], c.ones, c.P[g & 1][0]);
    else if constexpr (g < 4) l_mfma1(c.LT[g & 1], c.ones, c.P[g & 1][1]);
    else { constexpr int v = (g - 4) >> 1, qb = g & 1, db = v >> 1, ks = v & 1; pv_mfma<AO_(db, qb), AF_(32 + v)>(c.P[qb][ks]); }
    if constexpr ((g & 1) == 0 && g / 2 + 12 < 32) frag_rd<AF_(32 + g / 2 + 12), (g / 2 + 12) * 1024>(c.vb[X]);
    if constexpr (KIND != 0) step(IC<KIND>{}, G);
    if constexpr (g == 40) { pin(c.LT[0], c.LT[1]); c.l[0] += c.LT[0][0]; c.l[1] += c.LT[1][0]; pin(c.l[0], c.l[1]); }
    SB();
  });
  fin(IC<KIND>{});
}

struct P3 {
  srd_t srdW, srdP;
  unsigned vW[4];
  unsigned bw[3][4];
  unsigned bB[4];
  unsigned vlane, m0s[4], sW, pbase;
  float f[8];
};
constexpr int A3(int par, int nb, int qb) { return 32 * par + 8 * nb + 4 * qb; }
constexpr int R3(int F) { return 208 + 4 * (F % 12); }
constexpr int P3_LA = 16;
template <int A> DI float acc_rd1() { float r; asm volatile("v_accvgpr_read_b32 %0, a%c1" : "=v"(r) : "i"(A)); return r; }
DI void p3_issue_tile(P3& c, unsigned m0v, unsigned soff) {
  setm0(m0v); nop1();
  dma16s<0>(c.vW[0], c.srdW, soff); dma16s<1024>(c.vW[1], c.srdW, soff); dma16s<2048>(c.vW[2], c.srdW, soff); dma16s<3072>(c.vW[3], c.srdW, soff);
  setm0(m0v + 4096u); nop1();
  const unsigned s2 = soff + 4u * 7168u;
  dma16s<0>(c.vW[0], c.srdW, s2); dma16s<1024>(c.vW[1], c.srdW, s2); dma16s<2048>(c.vW[2], c.srdW, s2); dma16s<3072>(c.vW[3], c.srdW, s2);
}
template <int S> DI void p3_tile_step(P3& c, unsigned m0v, unsigned soff) {
  if constexpr (S == 0) setm0(m0v);
  else if constexpr (S <= 4) dma16o<1024 * (S - 1)>(c.vW[S - 1], c.srdW, soff);
  else if constexpr (S == 5) setm0(m0v + 4096u);
  else dma16o<1024 * (S - 6)>(c.vW[S - 6], c.srdW, soff + 4u * 7168u);
}
template <int CG, int F, int I> DI void p3_rd(P3& c) {
  constexpr int nb = F / 9, u = F % 9, reg = R3(F), sl = CG & 3;
  if constexpr (u < 8) tr_rd<reg + 2 * I, (sl == 1 ? 32768 : 0) + 4096 * u + 2048 * I>(c.bw[sl < 2 ? 0 : sl - 1][nb]);
  else tr_rd<reg + 2 * I, 1024 * CG + 16384 * I>(c.bB[nb]);
}
template <int CG>
DI void p3_group(P3& c, const u32x4 (&C)[2][8], const u32x4 (&OB)[2]) {
  constexpr int PAR = CG & 1;
  unsigned k0 = 0, k1 = 0, k2 = 0;
  sfor<72>([&](auto GG) {
    constexpr int g = GG, F = g >> 1, qb = g & 1, nb = F / 9, u = F % 9, reg = R3(F);
    if constexpr (qb == 0 && (F % 2) == 0) waitl<(CG == 6 && g + 16 > 72) ? (72 - g - 4 > 0 ? 72 - g - 4 : 0) : 12>();
    if constexpr (u < 8) mfK<A3(PAR, nb, qb), reg, u == 0>(C[qb][u]); else mfK<A3(PAR, nb, qb), reg, false>(OB[qb]);
    if constexpr (g == 40 && CG < 6) {
      constexpr int N = (CG + 2 <= 6 ? 8 : 0) + (CG >= 1 ? 4 : 0) + (CG >= 2 ? 4 : 0);
      waitv<(CG < 2 ? 63 : N)>(); BAR();
    }
    { constexpr int t = g + P3_LA, Fr = t >> 1, ir = t & 1;
      if constexpr (Fr < 36) p3_rd<CG, Fr, ir>(c); else if constexpr (CG < 6) p3_rd<CG + 1, Fr - 36, ir>(c); }
    if constexpr (CG + 3 <= 6) {
      constexpr int sl = (CG + 3) & 3;
      const unsigned so = c.sW + 128u * (CG + 3);
      if constexpr (g == 42) setm0(c.m0s[sl]);
      if constexpr (g == 43) dma16o<0>(c.vW[0], c.srdW, so);
      if constexpr (g == 45) dma16o<1024>(c.vW[1], c.srdW, so);
      if constexpr (g == 47) dma16o<2048>(c.vW[2], c.srdW, so);
      if constexpr (g == 49) dma16o<3072>(c.vW[3], c.srdW, so);
      if constexpr (g == 50) setm0(c.m0s[sl] + 4096u);
      if constexpr (g == 51) dma16o<0>(c.vW[0], c.srdW, so + 4u * 7168u);
      if constexpr (g == 53) dma16o<1024>(c.vW[1], c.srdW, so + 4u * 7168u);
      if constexpr (g == 55) dma16o<2048>(c.vW[2], c.srdW, so + 4u * 7168u);
      if constexpr (g == 57) dma16o<3072>(c.vW[3], c.srdW, so + 4u * 7168u);
    }
    if constexpr (CG >= 1) {
      sfor<4>([&](auto SS) {
        constexpr int s = SS, q2 = s >> 1, h2 = s & 1, g0 = 2 + 9 * s;
        if constexpr (g >= g0 && g < g0 + 8) { constexpr int e = g - g0; c.f[e] = acc_rd1<A3(PAR ^ 1, 2 * h2 + (e >> 2), q2) + (e & 3)>(); }
        if constexpr (g == g0 + 4) { k0 = cvtpk(c.f[0], c.f[1]); pin(k0); }
        if constexpr (g == g0 + 5) { k1 = cvtpk(c.f[2], c.f[3]); pin(k1); }
        if constexpr (g == g0 + 7) { k2 = cvtpk(c.f[4], c.f[5]); pin(k2); }
        if constexpr (g == g0 + 8) { const u32x4 pk = {k0, k1, k2, cvtpk(c.f[6], c.f[7])};
          st16<16>(c.srdP, c.vlane, c.pbase + (unsigned)((CG - 1) * 2048 + q2 * 14 * 1024 + h2 * 1024), pk); }
      });
    }
    SB();
  });
}

constexpr size_t FL_GEN = 0, FL_START = 4096, FL_KV = 8192, FL_Q = 16384, FL_O = 49152, FL_P = 81920;
typedef __attribute__((address_space(1))) unsigned gu32;
DI void flag_set(char* base, unsigned byteoff, unsigned tag) { __hip_atomic_store((gu32*)(base + byteoff), tag, __ATOMIC_RELAXED, __HIP_MEMORY_SCOPE_AGENT); }
DI unsigned flag_get(char* base, unsigned byteoff) { return __hip_atomic_load((gu32*)(base + byteoff), __ATOMIC_RELAXED, __HIP_MEMORY_SCOPE_AGENT); }
DI void ld_flag(unsigned& r, const char* p) { asm volatile("s_nop 4\n\tglobal_load_dword %0, %1, off sc1" : "=v"(r) : "v"(p) : "memory"); }
DI void poll_all(char* base, unsigned byteoff, unsigned tag) {
  unsigned spins = 0;
  while (!__all(flag_get(base, byteoff) == tag)) { if (++spins > (1u << 22)) break; __builtin_amdgcn_s_sleep(1); }
}

__global__ __launch_bounds__(256, 1) void fused_main(const bf16_t* __restrict__ x, const bf16_t* __restrict__ ckv, const bf16_t* __restrict__ wq, const bf16_t* __restrict__ wuk,
                                                    const bf16_t* __restrict__ wuv, const bf16_t* __restrict__ wo, const bf16_t* __restrict__ wqkv, const bf16_t* __restrict__ woB,
                                                    const bf16_t* __restrict__ gw, char* __restrict__ ws, bf16_t* __restrict__ out) {
  extern __shared__ __attribute__((aligned(16))) char smem[];
  LAS char* lds = (LAS char*)smem;
  const unsigned ldsb = (unsigned)(size_t)lds;
  asm volatile("" ::: ALL_AGPRS);
  unsigned long long code0; unsigned codelen;
  asm volatile("s_getpc_b64 %0\nfused_code_begin:\n\ts_mov_b32 %1, fused_code_end-fused_code_begin" : "=s"(code0), "=s"(codelen));
  STAMP_DECL; STAMP(0);
  const int b = blockIdx.x, xcd = b & 7, i = b >> 3;
  const int head = 2 * xcd + (i & 1);
  const unsigned lane = threadIdx.x & 63u, w = rfl(threadIdx.x >> 6);
  char* fl = ws + WS_FLAG;
  const srd_t srdH = mksrd(ws + (size_t)head * (1u << 20), 100u << 20);
  constexpr unsigned HQ = (unsigned)WS_Q, HPO = (unsigned)WS_PO;
  const unsigned hPART = (unsigned)WS_PART - (unsigned)head * (1u << 20);
  const unsigned hLSE = (unsigned)WS_LSE - (unsigned)head * (1u << 20);
  const unsigned gen = *(const __attribute__((address_space(4))) unsigned*)(fl + FL_GEN);
  const int isV = (i >> 1) & 1, tt = i >> 2;
  const unsigned tag = gen * 1664525u + 1013904223u;
  u32x4 kvp[32];
  P1 p1;
  const int pi = (i >> 1) & 7, role = i >> 4;
  constexpr int NA = NA_TILES;
  const int tl = 15 - pi, nl = 2 * (tl + 1);
  const int town = role ? pi : tl;
  {
    const unsigned wr = w >> 1, wc = w & 1u;
    XStream X; WStream W;
    X.srd = mksrd(ckv, SEQ * DC * 2);
#pragma unroll
    for (int j = 0; j < 8; ++j) X.v[j] = (8u * j + (lane >> 3)) * 2048u + 16u * ((lane & 7u) ^ (4u * (j & 1) + (lane >> 4))) - 1024u * (j & 3);
    X.soff = (256u * tt + 64u * w) * 2048u;
    makeW4(W, isV ? wuv : wuk, DC * 4096 * 2, 8192, head * 256, w, lane);
    unsigned xb[2], wb[8];
    {
      const unsigned r = lane & 15u, g = lane >> 4, q = (lane >> 2) & 3u, p = lane & 3u;
#pragma unroll
      for (int e = 0; e < 2; ++e) xb[e] = 128u * (128u * wr + r) + 16u * ((4u * e + g) ^ ((r >> 1) & 7u));
#pragma unroll
      for (int n = 0; n < 8; ++n) wb[n] = WBASE + 512u * (8u * g + q) + 32u * ((8u * wc + n) ^ ((q << 1) | (g & 1u))) + 8u * p;
    }
    issueX(X, ldsb + w * 8192u, 0);
    issueW(W, ldsb + WBASE + 0 * WSLOT + w * 4096u); issueX(X, ldsb + w * 8192u, 1);
    issueW(W, ldsb + WBASE + 1 * WSLOT + w * 4096u); issueX(X, ldsb + XSLOT + w * 8192u, 0);
    issueW(W, ldsb + WBASE + 2 * WSLOT + w * 4096u); issueX(X, ldsb + XSLOT + w * 8192u, 1);
    issueW(W, ldsb + WBASE + 3 * WSLOT + w * 4096u); issueX(X, ldsb + 2 * XSLOT + w * 8192u, 0);
    unsigned pj[1];
    { const srd_t scode = mksrd((const void*)code0, codelen);
      pf_load(pj[0], (w == 3u) ? ((unsigned)i * 64u + lane) * 128u : 0x7fffff00u, scode); }
    Prefetch pf;
    {
      const unsigned ci = (unsigned)i >> 1;
      pf.j0 = 0u; pf.so = 0u;
      if (w == 0) { pf.s0 = mksrd(wq, DM * 4096 * 2); pf.v0 = lane < 8u ? (28u * ci + (lane >> 2)) * 8192u + head * 512u + (lane & 3u) * 128u : 0x7fffff00u; pf.step = 16384u; }
      else if (w == 1) { pf.s0 = mksrd(wo, 4096 * DM * 2); pf.v0 = lane < 8u ? (unsigned)(head * 256 + 16 * (int)ci) * 896u + lane * 128u : 0x7fffff00u; pf.step = 1024u; }
      else if (w == 2) { pf.s0 = mksrd(wqkv, DM * 1344 * 2); pf.v0 = lane < 3u ? ((unsigned)b * 37u + lane) * 128u : 0x7fffff00u; pf.step = 384u; }
      else { pf.s0 = mksrd(x, SEQ * DM * 2); pf.v0 = lane < 4u ? ((unsigned)b * 56u + lane) * 128u : 0x7fffff00u; pf.step = 512u; }
    }
    { const u32x4 z = zero4(); nop1(); nop1(); sfor<16>([&](auto I) { clear16<16 * I>(z); }); }
    waitv<25>(); BAR();
    STAMP(1);
    Frags f0, f1;
#pragma unroll
    for (int n = 0; n < 8; ++n) { f0.wl[n] = readWh(lds, wb[n], 0); f0.wh[n] = readWh(lds, wb[n], 2048); }
#pragma unroll
    for (int m = 0; m < 8; ++m) f0.x[m] = readX(lds, xb[0], 2048 * m);
    f0.join();
    p1_setup(p1, x, wq, wqkv, head, town, w, lane);
    const srd_t sg = mksrd(gw, DM * 2);
    auto hook = [&](auto E4, auto I) { p1_preamble<decltype(E4)::value, decltype(I)::value>(p1, sg, ldsb, w, lane); };
    if (isV) kv_loop<true>(f0, f1, lds, ldsb, X, W, xb, wb, w, pf, hook);
    else kv_loop<false>(f0, f1, lds, ldsb, X, W, xb, wb, w, pf, hook);
    STAMP(2);
    pin(pf.j0); pin(pj[0]);
    snop<7>();
    {
      const unsigned wr2 = w >> 1, wc2 = w & 1u, vl = lane * 16u;
      const unsigned base = (isV ? (unsigned)WS_V : 0u) + (4u * tt + 2u * wr2) * 32768u + wc2 * (isV ? 16384u : 4096u);
      if (isV) sfor<32>([&](auto I) { constexpr int mp = I / 8, n = I % 8;
        if constexpr (I < KV_PRE) st16<16>(srdH, vl, base + (unsigned)((mp >> 1) * 32768 + n * 2048 + (mp & 1) * 1024), acc_pack1<4 * (8 * (2 * mp) + n), 4 * (8 * (2 * mp + 1) + n)>());
        else if constexpr (I == KV_PRE) kvp[I] = acc_pack1<4 * (8 * (2 * mp) + n), 4 * (8 * (2 * mp + 1) + n)>();
        else kvp[I] = acc_pack1n<4 * (8 * (2 * mp) + n), 4 * (8 * (2 * mp + 1) + n)>();
        SB(); });
      else sfor<32>([&](auto I) { constexpr int m = I / 4, np = I % 4;
        if constexpr (I < KV_PRE) st16<16>(srdH, vl, base + (unsigned)((m >> 2) * 32768 + (m & 3) * 8192 + np * 1024), acc_pack1<4 * (8 * m + 2 * np), 4 * (8 * m + 2 * np + 1)>());
        else if constexpr (I == KV_PRE) kvp[I] = acc_pack1<4 * (8 * m + 2 * np), 4 * (8 * m + 2 * np + 1)>();
        else kvp[I] = acc_pack1n<4 * (8 * m + 2 * np), 4 * (8 * m + 2 * np + 1)>();
        SB(); });
    }
    if (threadIdx.x == 0) flag_set(fl, FL_START + b * 4u, tag);
    STAMP(3);
  }
  P1Out po;
  unsigned kvflag = 0u;
  Att c;
  c.vlane = lane * 16u;
  {
    const unsigned wr = w >> 1, wc = w & 1u;
    const srd_t so = srdH;
    const unsigned base = (isV ? (unsigned)WS_V : 0u) + (4u * tt + 2u * wr) * 32768u + wc * (isV ? 16384u : 4096u);
    auto st = [&](auto I) {
      constexpr int ii = I;
      const unsigned off = isV ? (unsigned)(((ii / 8) >> 1) * 32768 + (ii % 8) * 2048 + ((ii / 8) & 1) * 1024) : (unsigned)(((ii / 4) >> 2) * 32768 + ((ii / 4) & 3) * 8192 + (ii % 4) * 1024);
      st16<KV_AUX>(so, c.vlane, base + off, kvp[ii]);
    };
    auto done = [&] { if (lane == 0) flag_set(fl, FL_KV + (unsigned)(head * 64 + (i >> 1) * 4 + (int)w) * 4u, tag); };
    auto early = [&] { ld_flag(kvflag, fl + FL_KV + (unsigned)(head * 64) * 4u + lane * 4u); };
    p1_loop<1>(p1, ldsb, w, lane, st, done, early ST_PASS);
  }
  STAMP(4);
  const bool split = nl > NA;
  const int nseg = role ? 2 : 1, seg0 = (role && !split) ? 1 : 0;
  const int k01 = role ? NA : 0, n1 = role ? (split ? nl - NA : 0) : (split ? NA : nl), n2 = role ? 2 * (pi + 1) : 0;
  c.srdK = mksrd(ws + WS_K + (size_t)head * (1u << 20), 1u << 20);
  c.srdV = mksrd(ws + WS_V + (size_t)head * (1u << 20), 1u << 20);
  { u32x4 o = {0x3f803f80u, 0x3f803f80u, 0x3f803f80u, 0x3f803f80u}; asm volatile("" : "+v"(o)); c.ones = o; }
  auto toff = [&](int jj) -> unsigned {
    unsigned t = (jj < n1) ? (unsigned)(k01 + jj) : (unsigned)(jj - n1);
    unsigned o = t * 32768u + w * 8192u;
    return (jj < n1 + n2) ? o : 0x40000000u;
  };
  const unsigned mK = ldsb + w * 8192u, mV = ldsb + 65536u + w * 8192u;
  auto dmaK8 = [&](int jj) { const unsigned so = toff(jj), m0 = mK + (jj & 1) * 32768u;
    setm0(m0); nop1(); sfor<4>([&](auto I) { dmaKs<I>(c, so); }); setm0(m0 + 4096u); nop1(); sfor<4>([&](auto I) { dmaKs<4 + I>(c, so); }); };
  auto dmaV8 = [&](int jj) { const unsigned so = toff(jj), m0 = mV + (jj & 1) * 32768u;
    setm0(m0); nop1(); sfor<4>([&](auto I) { dmaVs<I>(c, so); }); setm0(m0 + 4096u); nop1(); sfor<4>([&](auto I) { dmaVs<4 + I>(c, so); }); };
  auto loadQ = [&](int qt) {
    const unsigned so = HQ + (unsigned)(8 * qt + 2 * w) * 8192u;
    sfor<16>([&](auto I) { constexpr int qb = I >> 3, ds = I & 7; ld_acc16<AQ_(qb, ds), (I & 3) * 1024, 16>(c.vlane, srdH, so + (unsigned)(I >> 2) * 4096u); });
  };
  const unsigned fQ = FL_Q + (unsigned)((head * 8 + pi) * 4 + (int)w) * 64u, fO = FL_O + (unsigned)((head * 8 + pi) * 4 + (int)w) * 64u;
  auto poll1 = [&](unsigned off) { unsigned spins = 0; while (rfl(flag_get(fl, off)) != tag) { if (++spins > (1u << 22)) break; __builtin_amdgcn_s_sleep(1); } };
  waitv<0>(); pin(kvflag);
  if (!__all(kvflag == tag)) poll_all(fl, FL_KV + (unsigned)(head * 64) * 4u + lane * 4u, tag);
  BAR();
  dmaK8(0); dmaK8(1);
  STAMP(47);
  P1B pb;
  snop<7>(); snop<3>();
  p1_post_b1(pb, po, lane);
  {
    const unsigned so = HQ + (unsigned)(8 * town + 2 * (int)w) * 8192u;
    p1_post_q([&](auto I, const u32x4& v) { acc_wr4<AQ_(I >> 3, I & 7)>(v); st16<16>(srdH, c.vlane + (I & 3) * 1024u, so + (unsigned)(I >> 2) * 4096u, v); });
  }
  snop<1>();
  { const u32x4 z = zero4(); nop1(); nop1(); sfor<8>([&](auto I) { clear16<16 * I>(z); }); }
  STAMP(5);
  p1_post_b2(pb, po, town, w, lane);
  STAMP(45);
  waitv<0>();
  if (!role && split && lane == 0) flag_set(fl, fQ, tag);
  STAMP(44);
  if (role && split) { poll1(fQ); loadQ(tl); }
  STAMP(6);
  P3 p3;
  int jb = 0;
  bool pendO = false;
  for (int seg = seg0; seg < nseg; ++seg) {
    const int qt = seg ? town : tl, k0 = seg ? 0 : k01, n = seg ? n2 : n1;
    const bool lastseg = seg + 1 == nseg;
    const int q0 = 128 * qt + 32 * w;
    const unsigned par = jb & 1;
    c.kb[0] = ldsb + c.vlane + par * 32768u; c.kb[1] = ldsb + c.vlane + (par ^ 1) * 32768u;
    c.vb[0] = ldsb + c.vlane + 65536u + par * 32768u; c.vb[1] = ldsb + c.vlane + 65536u + (par ^ 1) * 32768u;
    c.l[0] = c.l[1] = 0.f;
    c.mref[0] = c.mref[1] = 0.f; c.fct[0] = c.fct[1] = 1.f; c.pend = false;
    const int dqb = q0 + (int)(lane & 15u) - 4 * (int)(lane >> 4);
    auto isdiag = [&](int t) { return (k0 + t) >= 2 * qt; };
    build_cin(c, isdiag(0), dqb - 64 * k0);
    if (seg == seg0) { waitv<0>(); BAR(); }
    sfor<NPRE>([&](auto I) { frag_rd<AF_(I), I * 1024>(c.kb[0]); });
    waitl<0>();
    SB();
    const unsigned so = HPO + (unsigned)(8 * qt + 2 * w) * 8192u;
    float* lsep = (float*)(ws + WS_LSE) + head * 2048 + q0 + (lane & 15u);
    float lseB[2] = {0.f, 0.f};
    unsigned oflag = 0u, vbB = 0u, sW = 0u, sWB = 0u, m0B = 0u, lse_a = 0u;
    srd_t sB = p3.srdW;
    auto dstart = [&](auto K) { if constexpr (K == 2) ld_flag(oflag, fl + fO); };
    auto dpre = [&](auto K) {
      if constexpr (K != 0) {
        if constexpr (K == 2) { pin(oflag); if (rfl(oflag) != tag) poll1(fO); }
        const unsigned g = lane >> 4, q = (lane >> 2) & 3u, p = lane & 3u;
        p3.srdW = mksrd(wo, 4096 * DM * 2);
#pragma unroll
        for (int jj = 0; jj < 4; ++jj) p3.vW[jj] = (8u * jj + (lane >> 3)) * 896u + 16u * ((lane & 7u) ^ (((lane >> 4) & 3u) << 1)) - 1024u * jj;
        sW = (unsigned)(head * 256 + 64 * (int)w) * 896u;
        sB = mksrd((const char*)woB - 4096, DM * DM * 2 + 4096);
        vbB = 4096u + (lane >> 3) * 896u + 16u * ((lane & 7u) ^ (((lane >> 4) & 3u) << 1));
        m0B = mV + ((jb + n) & 1) * 32768u; sWB = (unsigned)(28 * head + 8 * (int)w) * 896u;
        const unsigned r = 4u * g + q, swz = (r >> 1) & 3u;
        const unsigned vlast = 65536u + ((jb + n + 1) & 1) * 32768u;
#pragma unroll
        for (int nb = 0; nb < 4; ++nb) {
          const unsigned b0 = ldsb + 128u * r + 32u * ((unsigned)nb ^ swz) + 8u * p;
          p3.bw[0][nb] = b0; p3.bw[1][nb] = b0 + 131072u; p3.bw[2][nb] = b0 + vlast;
          p3.bB[nb] = ldsb + 65536u + ((jb + n) & 1) * 32768u + (r >> 3) * 8192u + (r & 7u) * 128u + 32u * ((unsigned)nb ^ swz) + 8u * p;
        }
        p3.m0s[0] = mK; p3.m0s[1] = mK + 32768u; p3.m0s[2] = ldsb + 131072u + w * 8192u; p3.m0s[3] = ldsb + vlast + w * 8192u;
        p3.sW = sW;
        lse_a = hLSE + (unsigned)(head * 2048 + q0 + (int)(lane & 15u)) * 4u;
      }
    };
    auto dstep = [&](auto K, auto G) {
      constexpr int k0_ = (int)G - 1 - (K == 2 ? 18 : 0);
      if constexpr (K == 2 && G >= 1 && G <= 18) {
        constexpr int j = (int)G - 1;
        if constexpr (j < 2) ld_f32c<64 * j>(lseB[j], lse_a, srdH);
        else { constexpr int I = j - 2, qb = I >> 3, u = I & 7; ld_acc16n<AQ_(qb, u), (u & 3) * 1024>(c.vlane, srdH, so + (unsigned)(2 * qb + (u >> 2)) * 4096u); }
      } else if constexpr (k0_ >= 0 && k0_ < 39) {
        if constexpr (k0_ < 10) p3_tile_step<k0_>(p3, p3.m0s[0], sW);
        else if constexpr (k0_ < 19) {
          constexpr int s = k0_ - 10;
          if constexpr (s == 0) setm0(m0B);
          else if constexpr (s <= 4) dma16o<1024 * (s - 1)>(vbB + (unsigned)(128 * (s - 1) - 1024 * (s - 1)), sB, sWB);
          else if constexpr (s == 5) setm0(m0B + 4096u);
          else dma16o<1024 * (s - 6)>(vbB + (unsigned)(128 * (s - 2) - 1024 * (s - 6)), sB, sWB);
        }
        else if constexpr (k0_ < 29) p3_tile_step<k0_ - 19>(p3, p3.m0s[1], sW + 128u);
        else p3_tile_step<k0_ - 29>(p3, p3.m0s[2], sW + 256u);
      }
    };
    auto dfin = [&](auto K) { if constexpr (K == 2) { waitv<31>(); pin(lseB[0], lseB[1]); } };
    auto run_drain = [&](auto XX) {
      constexpr int X_ = XX;
      if (!lastseg) drain<X_, 0>(c, dstart, dpre, dstep, dfin);
      else if (!role && split) drain<X_, 2>(c, dstart, dpre, dstep, dfin);
      else drain<X_, 1>(c, dstart, dpre, dstep, dfin);
    };
    auto nomid = [] {};
    phaseA<0, true>(c, [&] { if (pendO) { if (lane == 0) flag_set(fl, fO, tag); pendO = false; } });
    phaseB<0, true>(c, mK + ((jb + 2) & 1) * 32768u, toff(jb + 2), mV + (jb & 1) * 32768u, toff(jb));
    if (1 < n) { build_cin(c, isdiag(1), dqb - 64 * (k0 + 1)); }
    int t = 1;
    for (;;) {
      if (t >= n) { run_drain(IC<0>{}); break; }
      phaseA<1, false>(c, nomid);
      if (t == n - 1 && !lastseg) loadQ(town);
      phaseB<1, false>(c, mK + ((jb + t) & 1) * 32768u, toff(jb + t + 2), mV + ((jb + t) & 1) * 32768u, toff(jb + t));
      { const bool nd = (t + 1 < n) && isdiag(t + 1);
        if (__builtin_expect(c.pend || nd, 0)) { if (c.pend) rescale_O(c); build_cin(c, nd, dqb - 64 * (k0 + t + 1)); } }
      ++t;
      if (t >= n) { run_drain(IC<1>{}); break; }
      phaseA<0, false>(c, nomid);
      if (t == n - 1 && !lastseg) loadQ(town);
      phaseB<0, false>(c, mK + ((jb + t) & 1) * 32768u, toff(jb + t + 2), mV + ((jb + t) & 1) * 32768u, toff(jb + t));
      { const bool nd = (t + 1 < n) && isdiag(t + 1);
        if (__builtin_expect(c.pend || nd, 0)) { if (c.pend) rescale_O(c); build_cin(c, nd, dqb - 64 * (k0 + t + 1)); } }
      ++t;
    }
    if (n == 1 && !lastseg) loadQ(town);
    STAMP_D(7 + seg);
    asm volatile("s_nop 7" ::: "memory");
    if (!lastseg) {
      float cA[2];
#pragma unroll
      for (int qb = 0; qb < 2; ++qb) {
        cA[qb] = c.l[qb] > 0.f ? __builtin_amdgcn_rcpf(c.l[qb]) : 0.f;
        const float lse = c.l[qb] > 0.f ? c.mref[qb] + __builtin_amdgcn_logf(c.l[qb]) : -1e30f;
        if (lane < 16u) __hip_atomic_store(lsep + 16 * qb, lse, __ATOMIC_RELAXED, __HIP_MEMORY_SCOPE_AGENT);
      }
      sfor<16>([&](auto I) {
        constexpr int qb = I >> 3, u = I & 7;
        st16<16>(srdH, c.vlane + (u & 3) * 1024u, so + (unsigned)(2 * qb + (u >> 2)) * 4096u, acc_pack<AO_(2 * u, qb), AO_(2 * u + 1, qb)>(cA[qb]));
        SB();
      });
      pendO = true;
      snop<1>();
      { const u32x4 z = zero4(); nop1(); nop1(); sfor<8>([&](auto I) { clear16<16 * I>(z); }); }
    } else {
      float cA[2], cB[2] = {0.f, 0.f};
      if (!role && split) {
#pragma unroll
        for (int qb = 0; qb < 2; ++qb) {
          const float lseA = c.mref[qb] + __builtin_amdgcn_logf(c.l[qb]);
          const float M = fmaxf(lseA, lseB[qb]);
          const float wA = __builtin_amdgcn_exp2f(lseA - M), wB = __builtin_amdgcn_exp2f(lseB[qb] - M);
          const float it = po.g[qb] * __builtin_amdgcn_rcpf(wA + wB);
          cA[qb] = wA * it * __builtin_amdgcn_rcpf(c.l[qb]); cB[qb] = wB * it;
        }
      } else {
#pragma unroll
        for (int qb = 0; qb < 2; ++qb) cA[qb] = po.g[qb] * __builtin_amdgcn_rcpf(c.l[qb]);
      }
      u32x4 C[2][8];
      if (!role && split) {
        sfor<16>([&](auto I) {
          constexpr int qb = I >> 3, u = I & 7;
          float f0[4], f1[4], pb[4];
          acc_rd4n<AO_(2 * u, qb)>(f0); acc_rd4n<AO_(2 * u + 1, qb)>(f1); acc_rd4n<AQ_(qb, u)>(pb);
          float o[8];
#pragma unroll
          for (int r = 0; r < 4; ++r) { o[r] = f0[r] * cA[qb]; o[4 + r] = f1[r] * cA[qb]; }
#pragma unroll
          for (int d = 0; d < 4; ++d) {
            o[2 * d] += cB[qb] * __uint_as_float(__float_as_uint(pb[d]) << 16);
            o[2 * d + 1] += cB[qb] * __uint_as_float(__float_as_uint(pb[d]) & 0xffff0000u);
          }
          u32x4 v; v[0] = cvtpk(o[0], o[1]); v[1] = cvtpk(o[2], o[3]); v[2] = cvtpk(o[4], o[5]); v[3] = cvtpk(o[6], o[7]);
          C[qb][u] = v;
          SB();
        });
      } else {
        sfor<16>([&](auto I) { constexpr int qb = I >> 3, u = I & 7; C[qb][u] = acc_packn<AO_(2 * u, qb), AO_(2 * u + 1, qb)>(cA[qb]); SB(); });
      }
      STAMP(9);
      waitv<0>(); BAR();
      p3.srdP = srdH;
      p3.vlane = c.vlane;
      p3.pbase = hPART + (unsigned)head * (128u * 14u * 1024u) + (unsigned)(8 * qt + 2 * (int)w) * (14u * 1024u);
      sfor<16>([&](auto T) { p3_rd<0, (T >> 1), (T & 1)>(p3); });
      waitl<0>();
      sfor<16>([&](auto I) { pin(C[I >> 3][I & 7]); }); pin(po.ob[0], po.ob[1]); snop<1>();
      SB();
      STAMP(32);
      sfor<7>([&](auto CG) { p3_group<CG>(p3, C, po.ob); });
      asm volatile("s_nop 7" ::: "memory");
      sfor<4>([&](auto SS) {
        constexpr int q2 = SS >> 1, h2 = SS & 1;
        st16<16>(p3.srdP, p3.vlane, p3.pbase + 6u * 2048u + (unsigned)(q2 * 14 * 1024 + h2 * 1024), acc_pack1<A3(0, 2 * h2, q2), A3(0, 2 * h2 + 1, q2)>());
      });
    }
    jb += n;
  }
  waitv<0>();
  STAMP(10);
  if (lane == 0) flag_set(fl, FL_P + (unsigned)(town * 64 + head * 4 + (int)w) * 4u, tag);
  {
    const srd_t sp = srdH;
    u32x4 v[2][16];
    unsigned selLo = 0x00003f80u, selHi = 0x3f800000u; pin(selLo, selHi);
    const unsigned un0 = 7u * head + w, un1 = un0 + 4u;
    const unsigned o0 = hPART + ((8u * town + un0 / 14u) * 14u + un0 % 14u) * 1024u, o1 = (w < 3u) ? hPART + ((8u * town + un1 / 14u) * 14u + un1 % 14u) * 1024u : 0x40000000u;
    unsigned long long ready = __ballot(flag_get(fl, FL_P + (unsigned)(town * 64) * 4u + lane * 4u) == tag);
    unsigned spins = 0;
    float a[2][8] = {{0.f, 0.f, 0.f, 0.f, 0.f, 0.f, 0.f, 0.f}, {0.f, 0.f, 0.f, 0.f, 0.f, 0.f, 0.f, 0.f}};
    auto add = [&](int h) {
#pragma unroll
      for (int k = 0; k < 2; ++k)
#pragma unroll
        for (int d = 0; d < 4; ++d) { a[k][2 * d] = dot2bf(v[k][h][d], selLo, a[k][2 * d]); a[k][2 * d + 1] = dot2bf(v[k][h][d], selHi, a[k][2 * d + 1]); }
    };
#pragma unroll
    for (int h = 0; h < 16; ++h) {
      while (((ready >> (4 * h)) & 15ull) != 15ull) {
        if (++spins > (1u << 22)) break;
        __builtin_amdgcn_s_sleep(1);
        ready = __ballot(flag_get(fl, FL_P + (unsigned)(town * 64) * 4u + lane * 4u) == tag);
      }
      v[0][h] = __builtin_amdgcn_raw_buffer_load_b128(sp, lane * 16u, (unsigned)h * (128u * 14u * 1024u) + o0, 16);
      v[1][h] = __builtin_amdgcn_raw_buffer_load_b128(sp, lane * 16u, (unsigned)h * (128u * 14u * 1024u) + o1, 16);
      if (h >= 6) { SB(); add(h - 6); SB(); }
    }
    STAMP(48);
#pragma unroll
    for (int h = 10; h < 16; ++h) add(h);
#pragma unroll
    for (int k = 0; k < 2; ++k) {
      if (k == 1 && w == 3) break;
      const unsigned un = k ? un1 : un0, tb = 8u * town + un / 14u, u = un % 14u;
      bf16_t* o = out + (size_t)(16u * tb + (lane & 15u)) * DM + 32u * u + 4u * (lane >> 4);
      *(uint2*)o = make_uint2(cvtpk(a[k][0], a[k][1]), cvtpk(a[k][2], a[k][3]));
      *(uint2*)(o + 16) = make_uint2(cvtpk(a[k][4], a[k][5]), cvtpk(a[k][6], a[k][7]));
    }
  }
  if (b == 0 && w == 0) {
    for (int r = 0; r < 4; ++r) poll_all(fl, FL_START + (unsigned)(r * 64) * 4u + lane * 4u, tag);
    if (lane == 0) flag_set(fl, FL_GEN, tag);
  }
  STAMP(11); STAMP_OUT(1);
  asm volatile("fused_code_end:");
}

extern "C" void kernel_launch(void* const* d_in, const int* in_sizes, int n_in, void* d_out, int out_size, void* d_ws, size_t ws_size, hipStream_t stream) {
  static bool attr = false;
  if (!attr) { (void)hipFuncSetAttribute((const void*)fused_main, hipFuncAttributeMaxDynamicSharedMemorySize, LDS_BYTES); attr = true; }
  fused_main<<<256, 256, LDS_BYTES, stream>>>((const bf16_t*)d_in[0], (const bf16_t*)d_in[1], (const bf16_t*)d_in[2], (const bf16_t*)d_in[3], (const bf16_t*)d_in[4],
                                             (const bf16_t*)d_in[5], (const bf16_t*)d_in[7], (const bf16_t*)d_in[8], (const bf16_t*)d_in[9], (char*)d_ws, (bf16_t*)d_out);
}
```

```cpp
#include <hip/hip_runtime.h>
#include <stdint.h>
#include <utility>
#include <type_traits>

typedef unsigned short bf16_t;
typedef __bf16 bf16x8 __attribute__((ext_vector_type(8)));
typedef __bf16 bf16x4 __attribute__((ext_vector_type(4)));
typedef __bf16 bf16x2 __attribute__((ext_vector_type(2)));
typedef float f32x4 __attribute__((ext_vector_type(4)));
typedef float f32x2 __attribute__((ext_vector_type(2)));
typedef unsigned u32x4 __attribute__((ext_vector_type(4)));
typedef __amdgpu_buffer_rsrc_t srd_t;
#define LAS __attribute__((address_space(3)))
#define DI __device__ __forceinline__
#define SB() __builtin_amdgcn_sched_barrier(0)
#define ACL8(b) "a" #b "0","a" #b "1","a" #b "2","a" #b "3","a" #b "4","a" #b "5","a" #b "6","a" #b "7","a" #b "8","a" #b "9"
#define ALL_AGPRS "a0","a1","a2","a3","a4","a5","a6","a7","a8","a9", ACL8(1),ACL8(2),ACL8(3),ACL8(4),ACL8(5),ACL8(6),ACL8(7),ACL8(8),ACL8(9),ACL8(10),ACL8(11),ACL8(12), \
  ACL8(13),ACL8(14),ACL8(15),ACL8(16),ACL8(17),ACL8(18),ACL8(19),ACL8(20),ACL8(21),ACL8(22),ACL8(23),ACL8(24),"a250","a251","a252","a253","a254","a255"

constexpr int SEQ = 2048, DM = 448, DC = 1024;
constexpr size_t WS_K = 0, WS_V = 16u << 20, WS_Q = 32u << 20, WS_PO = 48u << 20, WS_PART = 64u << 20, WS_LSE = 96u << 20, WS_FLAG = 97u << 20;
constexpr float LOG2E = 1.4426950408889634f;
constexpr float QSCALE = 0.0625f * LOG2E;
constexpr float BSCALE = 0.1889822365046136f * LOG2E;
constexpr int LDS_BYTES = 163840;

DI unsigned cvtpk(float lo, float hi) { f32x2 v = {lo, hi}; bf16x2 b = __builtin_convertvector(v, bf16x2); return __builtin_bit_cast(unsigned, b); }
DI float dot2bf(unsigned a, unsigned b, float c) { return __builtin_amdgcn_fdot2_f32_bf16(__builtin_bit_cast(bf16x2, a), __builtin_bit_cast(bf16x2, b), c, false); }
DI unsigned rfl(unsigned x) { return __builtin_amdgcn_readfirstlane(x); }
DI srd_t mksrd(const void* p, unsigned bytes) { return __builtin_amdgcn_make_buffer_rsrc((void*)p, (short)0, (int)bytes, 0x00020000); }
template <int... Is, class F> DI void sfor_impl(std::integer_sequence<int, Is...>, F&& f) { (f(std::integral_constant<int, Is>{}), ...); }
template <int N, class F> DI void sfor(F&& f) { sfor_impl(std::make_integer_sequence<int, N>{}, f); }
template <int N> using IC = std::integral_constant<int, N>;

template <int OFF> DI void dma16o(unsigned voff, srd_t srd, unsigned soff) {
  asm volatile("buffer_load_dwordx4 %0, %1, %2 offen offset:%c3 lds" :: "v"(voff), "s"(srd), "s"(soff), "i"(OFF) : "memory");
}
template <int OFF> DI void dma16s(unsigned voff, srd_t srd, unsigned soff) {
  asm volatile("s_nop 4\n\tbuffer_load_dwordx4 %0, %1, %2 offen offset:%c3 lds" :: "v"(voff), "s"(srd), "s"(soff), "i"(OFF) : "memory");
}
template <int OFF> DI void dma16c(unsigned voff, srd_t srd, unsigned soff) {
  asm volatile("buffer_load_dwordx4 %0, %1, %2 offen offset:%c3 sc1 lds" :: "v"(voff), "s"(srd), "s"(soff), "i"(OFF) : "memory");
}
template <int OFF> DI void dma16cs(unsigned voff, srd_t srd, unsigned soff) {
  asm volatile("s_nop 4\n\tbuffer_load_dwordx4 %0, %1, %2 offen offset:%c3 sc1 lds" :: "v"(voff), "s"(srd), "s"(soff), "i"(OFF) : "memory");
}
DI void setm0(unsigned v) { asm volatile("s_mov_b32 m0, %0" :: "s"(v) : "memory"); }
DI void nop1() { asm volatile("s_nop 0"); }
template <int N> DI void waitv() { asm volatile("s_waitcnt vmcnt(%c0)" :: "i"(N) : "memory", ALL_AGPRS); }
template <int N> DI void waitvl() { asm volatile("s_waitcnt vmcnt(%c0) lgkmcnt(0)" :: "i"(N) : "memory", ALL_AGPRS); }
template <int N> DI void waitl() { asm volatile("s_waitcnt lgkmcnt(%c0)" :: "i"(N) : "memory"); }
template <class T> DI void pin(T& a) { asm volatile("" : "+v"(a)); }
template <class T, class... R> DI void pin(T& a, R&... r) { pin(a); pin(r...); }
template <int N> DI void snop() { asm volatile("s_nop %c0" :: "i"(N) : "memory"); }
#define BAR() __builtin_amdgcn_s_barrier()


template <int AD> DI void clear16(const u32x4& z) {
  asm volatile("v_mfma_f32_32x32x16_bf16 a[%c1:%c2], %0, %0, 0" :: "v"(z), "i"(AD), "i"(AD + 15) : ALL_AGPRS);
}
DI u32x4 zero4() { unsigned a, b, c, e; asm volatile("s_nop 1\n\tv_mov_b32 %0, 0\n\tv_mov_b32 %1, 0\n\tv_mov_b32 %2, 0\n\tv_mov_b32 %3, 0" : "=v"(a), "=v"(b), "=v"(c), "=v"(e)); u32x4 z = {a, b, c, e}; asm volatile("" : "+v"(z)); return z; }
template <int A> DI void acc_rd4(float (&f)[4]) {
  asm volatile("s_nop 1\n\tv_accvgpr_read_b32 %0, a%c4\n\tv_accvgpr_read_b32 %1, a%c5\n\tv_accvgpr_read_b32 %2, a%c6\n\tv_accvgpr_read_b32 %3, a%c7"
               : "=v"(f[0]), "=v"(f[1]), "=v"(f[2]), "=v"(f[3]) : "i"(A), "i"(A + 1), "i"(A + 2), "i"(A + 3) : ALL_AGPRS);
}
template <int A> DI void acc_rd4n(float (&f)[4]) {
  asm volatile("v_accvgpr_read_b32 %0, a%c4\n\tv_accvgpr_read_b32 %1, a%c5\n\tv_accvgpr_read_b32 %2, a%c6\n\tv_accvgpr_read_b32 %3, a%c7"
               : "=v"(f[0]), "=v"(f[1]), "=v"(f[2]), "=v"(f[3]) : "i"(A), "i"(A + 1), "i"(A + 2), "i"(A + 3) : ALL_AGPRS);
}
template <int A, int B> DI u32x4 acc_pack1n() {
  float f[4], g[4]; acc_rd4n<A>(f); acc_rd4n<B>(g);
  u32x4 v; v[0] = cvtpk(f[0], f[1]); v[1] = cvtpk(f[2], f[3]); v[2] = cvtpk(g[0], g[1]); v[3] = cvtpk(g[2], g[3]);
  return v;
}
template <int A, int B> DI u32x4 acc_packn(float sc) {
  float f[4], g[4]; acc_rd4n<A>(f); acc_rd4n<B>(g);
  u32x4 v; v[0] = cvtpk(f[0] * sc, f[1] * sc); v[1] = cvtpk(f[2] * sc, f[3] * sc); v[2] = cvtpk(g[0] * sc, g[1] * sc); v[3] = cvtpk(g[2] * sc, g[3] * sc);
  return v;
}
template <int A, int B> DI u32x4 acc_pack(float sc) {
  float f[4], g[4]; acc_rd4<A>(f); acc_rd4n<B>(g);
  u32x4 v; v[0] = cvtpk(f[0] * sc, f[1] * sc); v[1] = cvtpk(f[2] * sc, f[3] * sc); v[2] = cvtpk(g[0] * sc, g[1] * sc); v[3] = cvtpk(g[2] * sc, g[3] * sc);
  return v;
}
template <int A, int B> DI u32x4 acc_pack1() {
  float f[4], g[4]; acc_rd4<A>(f); acc_rd4n<B>(g);
  u32x4 v; v[0] = cvtpk(f[0], f[1]); v[1] = cvtpk(f[2], f[3]); v[2] = cvtpk(g[0], g[1]); v[3] = cvtpk(g[2], g[3]);
  return v;
}
template <int AUX> DI void st16(srd_t srd, unsigned voff, unsigned soff, u32x4 v) { __builtin_amdgcn_raw_buffer_store_b128(v, srd, voff, soff, AUX); }

constexpr int XSLOT = 32768, WBASE = 98304, WSLOT = 16384;
struct XStream { srd_t srd; unsigned v[8]; unsigned soff; };
struct WStream { srd_t srd; unsigned v[4]; unsigned soff, gstep; };
DI void issueX(XStream& X, unsigned ldsaddr, int half) {
  setm0(ldsaddr + half * 4096u); nop1();
  dma16o<0>(X.v[4 * half + 0], X.srd, X.soff); dma16o<1024>(X.v[4 * half + 1], X.srd, X.soff);
  dma16o<2048>(X.v[4 * half + 2], X.srd, X.soff); dma16o<3072>(X.v[4 * half + 3], X.srd, X.soff);
  if (half) X.soff += 128u;
}
DI void issueW(WStream& W, unsigned ldsaddr) {
  setm0(ldsaddr); nop1();
  dma16o<0>(W.v[0], W.srd, W.soff); dma16o<1024>(W.v[1], W.srd, W.soff); dma16o<2048>(W.v[2], W.srd, W.soff); dma16o<3072>(W.v[3], W.srd, W.soff);
  W.soff += W.gstep;
}
DI void makeW4(WStream& W, const void* p, unsigned bytes, unsigned ldw, unsigned col0, unsigned w, unsigned lane) {
  W.srd = mksrd(p, bytes);
#pragma unroll
  for (int j = 0; j < 4; ++j) W.v[j] = (2u * j + (lane >> 5)) * ldw + 16u * ((lane & 31u) ^ (((2u * (j & 1) + (lane >> 5)) << 2) | ((w & 1u) << 1))) - 1024u * j;
  W.soff = 8u * w * ldw + col0 * 2u; W.gstep = 32u * ldw;
}
struct Frags { bf16x8 x[8], w[8]; bf16x4 wl[8], wh[8];
  __device__ __forceinline__ void join() {
#pragma unroll
    for (int n = 0; n < 8; ++n) w[n] = __builtin_shufflevector(wl[n], wh[n], 0, 1, 2, 3, 4, 5, 6, 7);
  } };
template <int AD> DI void mfma16_a(const bf16x8& a, const bf16x8& b) {
  asm volatile("v_mfma_f32_16x16x32_bf16 a[%c2:%c3], %0, %1, a[%c2:%c3]" :: "v"(a), "v"(b), "i"(AD), "i"(AD + 3));
}
DI bf16x4 readWh(LAS char* lds, unsigned addr, int off) { return __builtin_amdgcn_ds_read_tr16_b64_v4bf16((LAS bf16x4*)(lds + addr + off)); }
DI bf16x8 readX(LAS char* lds, unsigned addr, int off) { return *(LAS bf16x8*)(lds + addr + off); }

struct Prefetch { srd_t s0; unsigned v0, j0, so, step; };
DI void pf_load(unsigned& j, unsigned voff, srd_t s) { asm volatile("s_nop 4\n\tbuffer_load_dword %0, %1, %2, 0 offen" : "=v"(j) : "v"(voff), "s"(s) : "memory"); }
DI void pf_slot(Prefetch& p) { asm volatile("buffer_load_dword %0, %1, %2, %3 offen" : "+v"(p.j0) : "v"(p.v0), "s"(p.s0), "s"(p.so) : "memory"); p.so += p.step; }
constexpr int kv_hook_ops(int e4) { return e4 == 1 ? 17 : (e4 == 2 ? 5 : (e4 == 3 ? 12 : 0)); }
template <bool VT, class Hook>
DI void kv_loop(Frags& f0, Frags& f1, LAS char* lds, unsigned ldsb, XStream& X, WStream& W, const unsigned (&xb)[2], const unsigned (&wb)[8], unsigned w, Prefetch& pf, Hook hook) {
  constexpr int S = 32;
  unsigned xs = 0;
  auto sub = [&](auto E4, auto LASTT, const Frags& cur, Frags& nxt) {
    constexpr int e4 = decltype(E4)::value, e = e4 & 1;
    constexpr bool LAST = decltype(LASTT)::value;
    constexpr int wo = e4 * WSLOT, won = ((e4 + 1) & 3) * WSLOT;
    unsigned xsn = xs; if (e == 1) xsn = (xs == 2 * XSLOT) ? 0u : xs + XSLOT;
    const unsigned xa = xb[(e + 1) & 1] + xsn;
    const unsigned xl = ldsb + ((e == 1) ? xs : ((xs == 0) ? 2 * XSLOT : xs - XSLOT)) + w * 8192u + ((e + 1) & 1) * 4096u;
    const unsigned wl = ldsb + WBASE + wo + w * 4096u;
    constexpr int hx = 4 * ((e + 1) & 1);
    sfor<64>([&](auto I) {
      constexpr int i = I, m = i / 8, n = i % 8;
      if constexpr (VT) mfma16_a<4 * (8 * m + n)>(cur.x[m], cur.w[n]); else mfma16_a<4 * (8 * m + n)>(cur.w[n], cur.x[m]);
      if constexpr (i == 1) {
        SB();
        if constexpr (!LAST) waitvl<(e == 0 ? 17 : 18)>(); else if constexpr (e4 == 0) waitvl<17>(); else if constexpr (e4 == 1) waitvl<0>(); else if constexpr (e4 == 2) waitvl<kv_hook_ops(1)>(); else waitvl<63>();
        BAR();
      }
      if constexpr (i >= 2 && !(LAST && e4 == 3)) {
        constexpr int k = i - 2;
        if constexpr (k == 0) nxt.wl[0] = readWh(lds, wb[0], won);
        else if constexpr (k == 1) nxt.wh[0] = readWh(lds, wb[0], won + 2048);
        else if constexpr (k == 2) nxt.x[0] = readX(lds, xa, 0);
        else if constexpr (k < 17) {
          constexpr int kk = k - 3, nn = 1 + kk / 2;
          if constexpr ((kk & 1) == 0) nxt.wl[nn] = readWh(lds, wb[nn], won); else nxt.wh[nn] = readWh(lds, wb[nn], won + 2048);
        } else if constexpr (k < 24) { constexpr int mm = 1 + k - 17; nxt.x[mm] = readX(lds, xa, 2048 * mm); }
      }
      if constexpr (!LAST) {
        constexpr int d0 = 26;
        if constexpr (i >= d0 && (i - d0) % 3 == 0 && (i - d0) / 3 < 10) {
          constexpr int d = (i - d0) / 3;
          if constexpr (d == 0) setm0(wl);
          else if constexpr (d == 1) dma16o<0>(W.v[0], W.srd, W.soff);
          else if constexpr (d == 2) dma16o<1024>(W.v[1], W.srd, W.soff);
          else if constexpr (d == 3) dma16o<2048>(W.v[2], W.srd, W.soff);
          else if constexpr (d == 4) dma16o<3072>(W.v[3], W.srd, W.soff);
          else if constexpr (d == 5) setm0(xl);
          else if constexpr (d == 6) dma16o<0>(X.v[hx + 0], X.srd, X.soff);
          else if constexpr (d == 7) dma16o<1024>(X.v[hx + 1], X.srd, X.soff);
          else if constexpr (d == 8) dma16o<2048>(X.v[hx + 2], X.srd, X.soff);
          else dma16o<3072>(X.v[hx + 3], X.srd, X.soff);
        }
        if constexpr (e == 0 && i == 57) pf_slot(pf);
      } else hook(E4, I);
      SB();
    });
    if constexpr (!LAST) { W.soff += W.gstep; if (((e + 1) & 1) == 1) X.soff += 128u; }
    xs = xsn;
    if constexpr (!(LAST && e4 == 3)) nxt.join();
  };
#pragma unroll 1
  for (int s = 0; s < S - 4; s += 4) {
    sub(IC<0>{}, std::false_type{}, f0, f1);
    sub(IC<1>{}, std::false_type{}, f1, f0);
    sub(IC<2>{}, std::false_type{}, f0, f1);
    if (s + 3 == S - 5) X.soff = 0x40000000u;
    sub(IC<3>{}, std::false_type{}, f1, f0);
  }
  sub(IC<0>{}, std::true_type{}, f0, f1);
  sub(IC<1>{}, std::true_type{}, f1, f0);
  sub(IC<2>{}, std::true_type{}, f0, f1);
  sub(IC<3>{}, std::true_type{}, f1, f0);
}

constexpr int AO_(int db, int qb) { return 8 * db + 4 * qb; }
constexpr int AQ_(int qb, int ds) { return 128 + 32 * qb + 4 * ds; }
constexpr int P1_WS = 24576, P1_XB = 98304, P1_XS = 20480, P1_G = 159744;
constexpr int p1_use(int f) { return f < 18 ? 2 * f : (f < 22 ? 36 + 3 * (f - 18) : 48); }
constexpr int P1_LA = 16, P1_R = 14, P1_NF = 14 * 23, P1_BAR = 21;
constexpr int p1_rd(int F) { return 50 * (F / 23) + p1_use(F % 23) - P1_LA; }
constexpr int p1_ni(int F) { return (F % 23) == 22 ? 1 : 2; }
constexpr int p1_reg(int F) { return 200 + 4 * (F % P1_R); }
constexpr int p1_xrd(int ks, int tb) { return 50 * (ks - 1) + 22 + 3 * tb; }
constexpr int p1_lds_between(int g0, bool after_w, int G) {
  int n = 0;
  for (int F = 0; F < P1_NF; ++F) for (int i = 0; i < p1_ni(F); ++i) { const int g = p1_rd(F) + i; if (g > g0 && g < G) ++n; }
  for (int ks = 1; ks < 14; ++ks) for (int tb = 0; tb < 3; ++tb) { const int g = p1_xrd(ks, tb); if ((g > g0 || (g == g0 && after_w)) && g < G) ++n; }
  return n;
}
constexpr bool p1_xiss(int ks) { return (ks & 1) == 1 && (ks + 5) / 2 < 7; }
constexpr int KV_PRE = 12;
constexpr int KV_LASTK = 10;
constexpr int p1_tot(int k) { return k < KV_LASTK ? ((32 - KV_PRE) * (k + 1) / KV_LASTK - (32 - KV_PRE) * k / KV_LASTK) : 0; }
constexpr int p1_nb(int k) { return p1_tot(k) > 0 ? 1 : 0; }
constexpr int p1_na(int k) { return p1_tot(k) - p1_nb(k); }
constexpr int p1_st0(int k) { int n = KV_PRE; for (int j = 0; j < k; ++j) n += p1_nb(j) + p1_na(j); return n; }
constexpr int p1_ops_before_barrier(int ks, int nst) {
  int n = 34 + (nst == 2 ? 32 : KV_PRE);
  for (int k = 0; k < ks; ++k) n += (nst == 1 ? 1 : 0) * (p1_nb(k) + p1_na(k)) + (k + 3 < 14 ? 6 : 0) + (p1_xiss(k) ? 5 : 0);
  return n + (nst == 1 ? 1 : 0) * p1_nb(ks);
}
constexpr int p1_wslot(int j) { return ((j + 2) % 4) * P1_WS; }
constexpr int p1_end_W(int j, int nst) {
  if (j == 0) return 11; if (j == 1) return 17; if (j == 2) return 29;
  return p1_ops_before_barrier(j - 3, nst) + 6;
}
constexpr int p1_end_x(int p, int nst) {
  if (p == 0) return 5; if (p == 1) return 22; if (p == 2) return 34;
  const int ks = 2 * p - 5;
  return p1_ops_before_barrier(ks, nst) + (ks + 3 < 14 ? 6 : 0) + 5;
}
constexpr int p1_wait(int ks, int nst) {
  int need = p1_end_W(ks + 1, nst);
  if (ks & 1) { const int e = p1_end_x((ks + 1) / 2, nst); if (e > need) need = e; }
  if (ks == 0 && need < 23) need = 23;
  return p1_ops_before_barrier(ks, nst) - need;
}
constexpr int ABq(int d, int qb) { return 128 + 8 * d + 4 * qb; }
constexpr int ABk(int d, int tb) { return 144 + 12 * d + 4 * tb; }
constexpr int ABv(int d, int tb) { return 168 + 12 * d + 4 * tb; }
constexpr int AZ(int qb) { return 192 + 4 * qb; }

template <int AD, int AF, bool Z> DI void mfK(const u32x4& x) {
  if constexpr (Z) asm volatile("v_mfma_f32_16x16x32_bf16 a[%c1:%c2], a[%c3:%c4], %0, 0" :: "v"(x), "i"(AD), "i"(AD + 3), "i"(AF), "i"(AF + 3));
  else asm volatile("v_mfma_f32_16x16x32_bf16 a[%c1:%c2], a[%c3:%c4], %0, a[%c1:%c2]" :: "v"(x), "i"(AD), "i"(AD + 3), "i"(AF), "i"(AF + 3));
}
template <int AD, int AF, bool Z> DI void mfV(const u32x4& x) {
  if constexpr (Z) asm volatile("v_mfma_f32_16x16x32_bf16 a[%c1:%c2], %0, a[%c3:%c4], 0" :: "v"(x), "i"(AD), "i"(AD + 3), "i"(AF), "i"(AF + 3));
  else asm volatile("v_mfma_f32_16x16x32_bf16 a[%c1:%c2], %0, a[%c3:%c4], a[%c1:%c2]" :: "v"(x), "i"(AD), "i"(AD + 3), "i"(AF), "i"(AF + 3));
}
template <int A, int OFF> DI void tr_rd(unsigned base) { asm volatile("ds_read_b64_tr_b16 a[%c1:%c2], %0 offset:%c3" :: "v"(base), "i"(A), "i"(A + 1), "i"(OFF) : "memory"); }
template <int A, int OFF> DI void frag_rd(unsigned base) { asm volatile("ds_read_b128 a[%c1:%c2], %0 offset:%c3" :: "v"(base), "i"(A), "i"(A + 3), "i"(OFF) : "memory"); }
template <int OFF> DI void lds_rdv(u32x4& r, unsigned base) { asm volatile("ds_read_b128 %0, %1 offset:%c2" : "=v"(r) : "v"(base), "i"(OFF) : "memory"); }
DI void mfma_vv0(f32x4& d, const u32x4& a, const u32x4& b) { asm volatile("v_mfma_f32_16x16x32_bf16 %0, %1, %2, 0" : "=&v"(d) : "v"(a), "v"(b)); }
DI void mfma_vvc(f32x4& d, const u32x4& a, const u32x4& b, const f32x4& c) { asm volatile("v_mfma_f32_16x16x32_bf16 %0, %1, %2, %3" : "=&v"(d) : "v"(a), "v"(b), "v"(c)); }
DI float xmax16_32(float v) {
  auto a = __builtin_amdgcn_permlane32_swap(__float_as_uint(v), __float_as_uint(v), false, false);
  v = fmaxf(__uint_as_float(a[0]), __uint_as_float(a[1]));
  auto b = __builtin_amdgcn_permlane16_swap(__float_as_uint(v), __float_as_uint(v), false, false);
  return fmaxf(__uint_as_float(b[0]), __uint_as_float(b[1]));
}
template <int A> DI void acc_wr4(const u32x4& v) {
  asm volatile("v_accvgpr_write_b32 a%c4, %0\n\tv_accvgpr_write_b32 a%c5, %1\n\tv_accvgpr_write_b32 a%c6, %2\n\tv_accvgpr_write_b32 a%c7, %3"
               :: "v"(v[0]), "v"(v[1]), "v"(v[2]), "v"(v[3]), "i"(A), "i"(A + 1), "i"(A + 2), "i"(A + 3) : ALL_AGPRS);
}

struct P1 {
  WStream W; srd_t sB, sX;
  unsigned vB[2], vX[5], sBo, sXo;
};
DI void p1_setup(P1& c, const bf16_t* x, const bf16_t* wq, const bf16_t* wqkv, int head, int T, unsigned w, unsigned lane) {
  makeW4(c.W, wq, DM * 4096 * 2, 8192, head * 256, w, lane);
  c.sB = mksrd(wqkv, DM * 1344 * 2);
#pragma unroll
  for (int jj = 0; jj < 2; ++jj) {
    const unsigned ch = (lane & 15u) ^ ((((lane >> 4) & 3u) << 2) | ((w & 1u) << 1)), sec = ch >> 2;
    c.vB[jj] = (sec == 3u) ? 0x7fffff00u : ((4u * jj + (lane >> 4)) * 2688u + sec * 896u + 56u * head + 16u * (ch & 3u) - 1024u * jj);
  }
  c.sBo = 8u * w * 2688u;
  c.sX = mksrd((const char*)x - 4096, SEQ * DM * 2 + 4096);
#pragma unroll
  for (int j = 0; j < 5; ++j) {
    const int row = 40 * (int)w + 8 * j + (int)(lane >> 3), tok = 128 * T - 16 + row;
    const unsigned sw = (4u * (w + j) + (lane >> 4)) & 7u;
    c.vX[j] = (tok < 0 || tok >= SEQ) ? 0x7fffff00u : (4096u + (unsigned)tok * 896u + 16u * ((lane & 7u) ^ sw) - 1024u * (j & 3));
  }
  c.sXo = 0u;
}
template <int STEP> DI void p1_W_step(P1& c, unsigned ldsb, int slot_off, unsigned w) {
  if constexpr (STEP == 0) setm0(ldsb + slot_off + w * 4096u);
  else if constexpr (STEP <= 4) dma16o<1024 * (STEP - 1)>(c.W.v[STEP - 1], c.W.srd, c.W.soff);
  else if constexpr (STEP == 5) setm0(ldsb + slot_off + 16384u + w * 2048u);
  else if constexpr (STEP == 6) dma16o<0>(c.vB[0], c.sB, c.sBo);
  else { dma16o<1024>(c.vB[1], c.sB, c.sBo); c.W.soff += c.W.gstep; c.sBo += 32u * 2688u; }
}
template <int STEP> DI void p1_x_step(P1& c, unsigned ldsb, int slot_off, unsigned w) {
  if constexpr (STEP == 0) setm0(ldsb + slot_off + w * 5120u);
  else if constexpr (STEP <= 4) dma16o<1024 * (STEP - 1)>(c.vX[STEP - 1], c.sX, c.sXo);
  else if constexpr (STEP == 5) setm0(ldsb + slot_off + w * 5120u + 4096u);
  else { dma16o<0>(c.vX[4], c.sX, c.sXo); c.sXo += 128u; }
}
template <int E4, int I> DI void p1_preamble(P1& c, srd_t sg, unsigned ldsb, unsigned w, unsigned lane) {
  if constexpr (I >= 4 && (I & 1) == 0) {
    constexpr int k = (I - 4) / 2;
    if constexpr (E4 == 1) {
      if constexpr (k < 7) p1_x_step<k>(c, ldsb, P1_XB + 0 * P1_XS, w);
      else if constexpr (k < 15) p1_W_step<k - 7>(c, ldsb, p1_wslot(0), w);
      else if constexpr (k < 23) p1_W_step<k - 15>(c, ldsb, p1_wslot(1), w);
    } else if constexpr (E4 == 2) {
      if constexpr (k < 7) p1_x_step<k>(c, ldsb, P1_XB + 1 * P1_XS, w);
    } else if constexpr (E4 == 3) {
      if constexpr (k == 0) setm0(ldsb + P1_G + w * 1024u);
      else if constexpr (k == 1) dma16o<0>(lane * 16u, sg, 0u);
      else if constexpr (k < 10) p1_W_step<k - 2>(c, ldsb, p1_wslot(2), w);
      else if constexpr (k < 17) p1_x_step<k - 10>(c, ldsb, P1_XB + 2 * P1_XS, w);
    }
  }
}

struct P1Out { float g[2]; u32x4 ob[2]; };

template <int NST, class St, class Done, class Early>
DI void p1_loop(P1& c, unsigned ldsb, unsigned w, unsigned lane, St st, Done done, Early early) {
  const unsigned r16 = lane & 15u, g = lane >> 4, q = (lane >> 2) & 3u, p = lane & 3u;
  unsigned bq[2][8], bb[2][6], bx[2];
  const unsigned fz = (q << 1) | (g & 1u);
#pragma unroll
  for (int s = 0; s < 2; ++s) {
#pragma unroll
    for (int n = 0; n < 8; ++n) bq[s][n] = ldsb + s * 2 * P1_WS + 512u * (8u * g + q) + 32u * ((unsigned)n ^ fz) + 8u * p;
#pragma unroll
    for (int n = 0; n < 6; ++n) bb[s][n] = ldsb + s * 2 * P1_WS + 16384u + 256u * (8u * g + q) + 32u * ((unsigned)n ^ fz) + 8u * p;
    bx[s] = ldsb + P1_XB + 128u * (32u * w + r16) + 16u * ((4u * s + g) ^ ((r16 >> 1) & 7u));
  }
  const unsigned bg = ldsb + P1_G + w * 1024u + 16u * g;
  u32x4 xa[2][3];
  auto rd_instr = [&](auto FF, auto II) {
    constexpr int F = FF, i = II, ks = F / 23, f = F % 23, sl = (ks + 2) % 4, set = sl / 2, so = (sl % 2) * P1_WS, reg = p1_reg(F);
    if constexpr (f < 16) tr_rd<reg + 2 * i, so + 256 * (f >> 3) + 2048 * i>(bq[set][f & 7]);
    else if constexpr (f < 22) tr_rd<reg + 2 * i, so + 1024 * i>(bb[set][f - 16]);
    else frag_rd<reg, 64 * ks>(bg);
  };
  auto rd_x = [&](auto KS, auto TB) { constexpr int ks = KS, tb = TB; lds_rdv<((ks >> 1) % 3) * P1_XS + 2048 * tb>(xa[ks & 1][tb], bx[ks & 1]); };
  waitv<34 - 11 + (NST == 2 ? 32 : KV_PRE)>();
  BAR();
  sfor<3>([&](auto TB) { rd_x(IC<0>{}, TB); });
  sfor<8>([&](auto FF) { rd_instr(FF, IC<0>{}); rd_instr(FF, IC<1>{}); });
  waitl<0>();
  pin(xa[0][0], xa[0][1], xa[0][2]);
  SB();
  sfor<14>([&](auto KS) {
    constexpr int ks = KS;
    sfor<50>([&](auto GG) {
      constexpr int gg = GG, G = 50 * ks + gg;
      constexpr int f = gg < 36 ? gg / 2 : (gg < 48 ? 18 + (gg - 36) / 3 : 22), sub = gg - p1_use(f), F = 23 * ks + f, reg = p1_reg(F);
      constexpr bool Z = ks == 0;
      if constexpr (gg == 0 && ks > 0) {
        constexpr int n = p1_lds_between(p1_xrd(ks, 2), false, G);
        waitl<(n > 15 ? 15 : n)>();
        pin(xa[ks & 1][0], xa[ks & 1][1], xa[ks & 1][2]);
      }
      if constexpr (sub == 0 && (F % 2) == 0 && p1_rd(F) >= 0) {
        constexpr int FN = (F + 1 < P1_NF) ? F + 1 : F;
        constexpr int n = p1_lds_between(p1_rd(FN) + p1_ni(FN) - 1, true, G);
        static_assert(p1_rd(FN) + p1_ni(FN) <= G, "read too late");
        waitl<(n > 15 ? 15 : n)>();
      }
      if constexpr (f < 16) mfK<AO_(f, sub), reg, Z>(xa[ks & 1][1 + sub]);
      else if constexpr (f < 18) mfK<ABq(f - 16, sub), reg, Z>(xa[ks & 1][1 + sub]);
      else if constexpr (f < 20) mfK<ABk(f - 18, sub), reg, Z>(xa[ks & 1][sub]);
      else if constexpr (f < 22) mfV<ABv(f - 20, sub), reg, Z>(xa[ks & 1][sub]);
      else mfK<AZ(sub), reg, Z>(xa[ks & 1][1 + sub]);
      if constexpr (gg == P1_BAR && ks < 13) {
        constexpr int N = p1_wait(ks, NST);
        static_assert(N >= 0 && N < 63, "vmcnt range");
        if constexpr (ks == KV_LASTK + 1) { waitv<0>(); BAR(); done(); }
        else { waitv<N>(); BAR(); }
      }
      sfor<12>([&](auto DD) {
        constexpr int H = 23 * ks + f + (int)DD;
        if constexpr (H < P1_NF) {
          if constexpr (p1_rd(H) == G) rd_instr(IC<H>{}, IC<0>{});
          else if constexpr (p1_ni(H) == 2 && p1_rd(H) + 1 == G) rd_instr(IC<H>{}, IC<1>{});
        }
      });
      if constexpr (ks < 13) { sfor<3>([&](auto TB) { if constexpr (p1_xrd(ks + 1, TB) == G) rd_x(IC<ks + 1>{}, TB); }); }
      if constexpr (ks + 3 < 14) {
        constexpr int so = p1_wslot(ks + 3);
        if constexpr (gg == 23) setm0(ldsb + so + w * 4096u);
        if constexpr (gg == 24) dma16o<0>(c.W.v[0], c.W.srd, c.W.soff);
        if constexpr (gg == 26) dma16o<1024>(c.W.v[1], c.W.srd, c.W.soff);
        if constexpr (gg == 27) dma16o<2048>(c.W.v[2], c.W.srd, c.W.soff);
        if constexpr (gg == 29) dma16o<3072>(c.W.v[3], c.W.srd, c.W.soff);
        if constexpr (gg == 30) setm0(ldsb + so + 16384u + w * 2048u);
        if constexpr (gg == 31) dma16o<0>(c.vB[0], c.sB, c.sBo);
        if constexpr (gg == 33) dma16o<1024>(c.vB[1], c.sB, c.sBo);
        if constexpr (gg == 34) { c.W.soff += c.W.gstep; c.sBo += 32u * 2688u; }
      }
      if constexpr (p1_xiss(ks)) {
        constexpr int so = P1_XB + (((ks + 5) / 2) % 3) * P1_XS;
        if constexpr (gg == 35) setm0(ldsb + so + w * 5120u);
        if constexpr (gg == 36) dma16o<0>(c.vX[0], c.sX, c.sXo);
        if constexpr (gg == 38) dma16o<1024>(c.vX[1], c.sX, c.sXo);
        if constexpr (gg == 39) dma16o<2048>(c.vX[2], c.sX, c.sXo);
        if constexpr (gg == 41) dma16o<3072>(c.vX[3], c.sX, c.sXo);
        if constexpr (gg == 42) setm0(ldsb + so + w * 5120u + 4096u);
        if constexpr (gg == 43) dma16o<0>(c.vX[4], c.sX, c.sXo);
        if constexpr (gg == 44) c.sXo += 128u;
      }
      if constexpr (NST == 1) {
        constexpr int b0 = p1_st0(ks), nb = p1_nb(ks), na = p1_na(ks);
        if constexpr (gg == 4 && nb >= 1) st(IC<(b0 < 32 ? b0 : 0)>{});
        if constexpr (gg == 14 && nb >= 2) st(IC<(b0 + 1 < 32 ? b0 + 1 : 0)>{});
        if constexpr (gg == 38 && na >= 3) st(IC<(b0 + nb < 32 ? b0 + nb : 0)>{});
        if constexpr (gg == 43 && na >= 2) st(IC<(b0 + nb + (na >= 3 ? 1 : 0) < 32 ? b0 + nb + (na >= 3 ? 1 : 0) : 0)>{});
        if constexpr (gg == 48 && na >= 1) st(IC<(b0 + nb + na - 1 < 32 && na >= 1 ? b0 + nb + na - 1 : 0)>{});
      }
      if constexpr (ks == 13 && gg == 24) early();
      SB();
    });
  });
}

struct P1B { u32x4 kf[3], vf[2][2], qf[2]; float omg[2]; };
DI void p1_post_b1(P1B& B, P1Out& o, unsigned lane) {
  const unsigned g = lane >> 4;
  float (&omg)[2] = B.omg;
#pragma unroll
  for (int qb = 0; qb < 2; ++qb) {
    float f[4];
    if (qb == 0) acc_rd4n<AZ(0)>(f); else acc_rd4n<AZ(1)>(f);
    const float e = __builtin_amdgcn_exp2f(-LOG2E * f[0]);
    o.g[qb] = __builtin_amdgcn_rcpf(1.f + e);
    omg[qb] = 1.f - o.g[qb];
  }
  {
    u32x4 (&kf)[3] = B.kf; u32x4 (&vf)[2][2] = B.vf; u32x4 (&qf)[2] = B.qf;
    sfor<3>([&](auto TB) { kf[TB] = acc_pack1n<ABk(0, TB), ABk(1, TB)>(); });
    sfor<2>([&](auto QB) {
      qf[QB] = acc_packn<ABq(0, QB), ABq(1, QB)>(BSCALE);
      if (g == 3u) { qf[QB][2] = 0u; qf[QB][3] = 0u; }
      sfor<2>([&](auto D) { vf[QB][D] = acc_pack1n<ABv(D, QB), ABv(D, QB + 1)>(); });
    });
  }
}
DI void p1_post_b2(P1B& B, P1Out& o, int T, unsigned w, unsigned lane) {
  const unsigned r16 = lane & 15u, g = lane >> 4;
  const int T0 = 128 * T + 32 * (int)w;
  {
    u32x4 (&kf)[3] = B.kf; u32x4 (&vf)[2][2] = B.vf; u32x4 (&qf)[2] = B.qf; float (&omg)[2] = B.omg;
    u32x4 ones = {0x3f803f80u, 0x3f803f80u, 0x3f803f80u, 0x3f803f80u};
    f32x4 cin[2][2], s[2][2];
#pragma unroll
    for (int qb = 0; qb < 2; ++qb)
#pragma unroll
      for (int kk = 0; kk < 2; ++kk)
#pragma unroll
        for (int r = 0; r < 4; ++r) {
          const int diff = (int)r16 - 4 * (int)g - r + 16 * (1 - kk);
          const int key = T0 + 16 * qb + (int)r16 - diff;
          cin[qb][kk][r] = (diff >= 0 && diff <= 14 && (diff & 1) == 0 && key >= 0) ? 0.f : -__builtin_inff();
        }
    pin(cin[0][0], cin[0][1], cin[1][0], cin[1][1], qf[0], qf[1], kf[0], kf[1], kf[2], ones); snop<1>();
    mfma_vvc(s[0][0], kf[0], qf[0], cin[0][0]); mfma_vvc(s[0][1], kf[1], qf[0], cin[0][1]);
    mfma_vvc(s[1][0], kf[1], qf[1], cin[1][0]); mfma_vvc(s[1][1], kf[2], qf[1], cin[1][1]);
    snop<9>(); pin(s[0][0], s[0][1], s[1][0], s[1][1]);
    u32x4 pf[2];
#pragma unroll
    for (int qb = 0; qb < 2; ++qb) {
      float m = fmaxf(fmaxf(fmaxf(s[qb][0][0], s[qb][0][1]), fmaxf(s[qb][0][2], s[qb][0][3])), fmaxf(fmaxf(s[qb][1][0], s[qb][1][1]), fmaxf(s[qb][1][2], s[qb][1][3])));
      m = xmax16_32(m);
      pf[qb][0] = cvtpk(__builtin_amdgcn_exp2f(s[qb][0][0] - m), __builtin_amdgcn_exp2f(s[qb][0][1] - m)); pf[qb][1] = cvtpk(__builtin_amdgcn_exp2f(s[qb][0][2] - m), __builtin_amdgcn_exp2f(s[qb][0][3] - m));
      pf[qb][2] = cvtpk(__builtin_amdgcn_exp2f(s[qb][1][0] - m), __builtin_amdgcn_exp2f(s[qb][1][1] - m)); pf[qb][3] = cvtpk(__builtin_amdgcn_exp2f(s[qb][1][2] - m), __builtin_amdgcn_exp2f(s[qb][1][3] - m));
    }
    f32x4 o0[2], o1[2], ls[2];
    pin(pf[0], pf[1], vf[0][0], vf[0][1], vf[1][0], vf[1][1]); snop<1>();
    mfma_vv0(o0[0], vf[0][0], pf[0]); mfma_vv0(o1[0], vf[0][1], pf[0]); mfma_vv0(ls[0], ones, pf[0]);
    mfma_vv0(o0[1], vf[1][0], pf[1]); mfma_vv0(o1[1], vf[1][1], pf[1]); mfma_vv0(ls[1], ones, pf[1]);
    snop<9>(); pin(o0[0], o1[0], ls[0], o0[1], o1[1], ls[1]);
#pragma unroll
    for (int qb = 0; qb < 2; ++qb) {
      const float sc = omg[qb] * __builtin_amdgcn_rcpf(ls[qb][0]);
      u32x4 ob; ob[0] = cvtpk(o0[qb][0] * sc, o0[qb][1] * sc); ob[1] = cvtpk(o0[qb][2] * sc, o0[qb][3] * sc); ob[2] = cvtpk(o1[qb][0] * sc, o1[qb][1] * sc); ob[3] = cvtpk(o1[qb][2] * sc, o1[qb][3] * sc);
      if (g == 3u) { ob[2] = 0u; ob[3] = 0u; }
      o.ob[qb] = ob;
    }
  }
}
template <class QSt> DI void p1_post_q(QSt qst) {
  sfor<16>([&](auto I) {
    constexpr int qb = I >> 3, ds = I & 7;
    const u32x4 v = acc_pack<AO_(2 * ds, qb), AO_(2 * ds + 1, qb)>(QSCALE);
    qst(I, v);
    SB();
  });
}
constexpr float THR = 8.f;
constexpr int AF_(int seq) { return 192 + 4 * (seq & 15); }

struct Att {
  f32x4 S[2][4][2];
  f32x4 CIN[4][2];
  u32x4 P[2][2];
  f32x4 LT[2];
  float l[2];
  u32x4 ones;
  float mref[2], fct[2], mx, mx2;
  unsigned kb[2], vb[2];
  unsigned vlane;
  srd_t srdK, srdV;
  bool pend;
};
template <int AF, int AQ> DI void qk_first(f32x4& s, const f32x4& c) {
  asm volatile("v_mfma_f32_16x16x32_bf16 %0, a[%c2:%c3], a[%c4:%c5], %1" : "=&v"(s) : "v"(c), "i"(AF), "i"(AF + 3), "i"(AQ), "i"(AQ + 3));
}
template <int AF, int AQ> DI void qk_next(f32x4& s) {
  asm volatile("v_mfma_f32_16x16x32_bf16 %0, a[%c1:%c2], a[%c3:%c4], %0" : "+v"(s) : "i"(AF), "i"(AF + 3), "i"(AQ), "i"(AQ + 3));
}
template <int AD, int AF> DI void pv_mfma(const u32x4& p) {
  asm volatile("v_mfma_f32_16x16x32_bf16 a[%c1:%c2], a[%c3:%c4], %0, a[%c1:%c2]" :: "v"(p), "i"(AD), "i"(AD + 3), "i"(AF), "i"(AF + 3));
}
DI void l_mfma0(f32x4& l, const u32x4& ones, const u32x4& p) {
  asm volatile("v_mfma_f32_16x16x32_bf16 %0, %1, %2, 0" : "=&v"(l) : "v"(ones), "v"(p));
}
DI void l_mfma1(f32x4& l, const u32x4& ones, const u32x4& p) {
  asm volatile("v_mfma_f32_16x16x32_bf16 %0, %1, %2, %0" : "+v"(l) : "v"(ones), "v"(p));
}
DI float vexp(float x) { float r = __builtin_amdgcn_exp2f(x); asm volatile("" : "+v"(r)); return r; }
DI float vmax3(float a, float b, float c) { float r = __builtin_elementwise_maximum(__builtin_elementwise_maximum(a, b), c); asm volatile("" : "+v"(r)); return r; }
DI unsigned vcvt(float lo, float hi) { unsigned r = cvtpk(lo, hi); asm volatile("" : "+v"(r)); return r; }

constexpr int NPRE = 8;
constexpr int rdgap(int seq) { return (seq < 32 ? 2 * seq : 68 + 2 * (seq - 32)) - 16; }
constexpr int usegap(int seq) { return seq < 32 ? 2 * seq : 68 + 2 * (seq - 32); }
constexpr int nafter(int seq, int g) {
  int n = 0;
  for (int s = 0; s < 64; ++s) for (int p = -1; p <= 1; ++p) { const int r = rdgap(s) + 132 * p; if (r > rdgap(seq) && r < g) ++n; }
  return n;
}
constexpr int WG = 4;
template <int G> DI void maybe_wait() {
  constexpr int seq = (G < 64) ? ((G & 1) == 0 ? G / 2 : -1) : (G >= 68 && (G & 1) == 0 ? 32 + (G - 68) / 2 : -1);
  if constexpr (seq >= 0 && (seq % WG) == 0) {
    constexpr int n = nafter(seq + WG - 1, G);
    static_assert(rdgap(seq + WG - 1) < G, "fragment not yet requested");
    waitl<(n > 15 ? 15 : n)>();
  }
}
template <int G, int X> DI void do_reads(Att& c) {
  sfor<64>([&](auto S_) {
    constexpr int s = S_;
    if constexpr (rdgap(s) == G) {
      if constexpr (s < 32) frag_rd<AF_(s), s * 1024>(c.kb[X]); else frag_rd<AF_(s), (s - 32) * 1024>(c.vb[X ^ 1]);
    } else if constexpr (rdgap(s) + 132 == G) frag_rd<AF_(s), s * 1024>(c.kb[X ^ 1]);
  });
}

template <int X, int H, int E> DI void do_exp(Att& c) {
  constexpr int kb = 2 * H + (E >> 3), qb = (E >> 2) & 1, r = E & 3; c.S[X][kb][qb][r] = vexp(c.S[X][kb][qb][r]);
}
template <int X, int KS, int PP> DI void do_pack(Att& c) {
  constexpr int kbl = PP >> 2, qb = (PP >> 1) & 1, h = PP & 1, kb = 2 * KS + kbl;
  c.P[qb][KS][2 * kbl + h] = vcvt(c.S[X][kb][qb][2 * h], c.S[X][kb][qb][2 * h + 1]);
}
template <int X, int I> DI void do_max(Att& c) {
  constexpr int ch = I & 1, st = I >> 1, base = 16 * ch;
  auto SV = [&](auto J) -> float { constexpr int j = J; return c.S[X][j >> 3][(j >> 2) & 1][j & 3]; };
  float& m = ch ? c.mx2 : c.mx;
  if constexpr (st == 0) m = vmax3(SV(std::integral_constant<int, base>{}), SV(std::integral_constant<int, base + 1>{}), SV(std::integral_constant<int, base + 2>{}));
  else if constexpr (st < 7) m = vmax3(m, SV(std::integral_constant<int, base + 2 * st + 1>{}), SV(std::integral_constant<int, base + 2 * st + 2>{}));
  else if constexpr (ch == 0) m = vmax3(m, SV(std::integral_constant<int, base + 15>{}), SV(std::integral_constant<int, base + 15>{}));
  else m = vmax3(m, SV(std::integral_constant<int, base + 15>{}), c.mx);
}
template <int X, bool FIRSTT> DI void raise_max(Att& c) {
  asm volatile("s_nop 7" ::: "memory");
#pragma unroll
  for (int qb = 0; qb < 2; ++qb) {
    float m = c.S[X][0][qb][0];
#pragma unroll
    for (int kb = 0; kb < 4; ++kb)
#pragma unroll
      for (int r = 0; r < 4; ++r) m = fmaxf(m, c.S[X][kb][qb][r]);
    m = xmax16_32(m);
    float dl = FIRSTT ? fmaxf(m, -1e30f) : fmaxf(m, 0.f);
    c.mref[qb] += dl;
    c.fct[qb] = FIRSTT ? 1.f : c.fct[qb] * __builtin_amdgcn_exp2f(-dl);
#pragma unroll
    for (int kb = 0; kb < 4; ++kb)
#pragma unroll
      for (int r = 0; r < 4; ++r) c.S[X][kb][qb][r] -= dl;
  }
  if (!FIRSTT) c.pend = true;
}
DI void build_cin(Att& c, bool diag, int dq) {
  if (diag) {
#pragma unroll
    for (int kb = 0; kb < 4; ++kb)
#pragma unroll
      for (int qb = 0; qb < 2; ++qb)
#pragma unroll
        for (int r = 0; r < 4; ++r) c.CIN[kb][qb][r] = (16 * kb + r - 16 * qb > dq) ? -__builtin_inff() : -c.mref[qb];
  } else {
#pragma unroll
    for (int kb = 0; kb < 4; ++kb)
#pragma unroll
      for (int qb = 0; qb < 2; ++qb)
#pragma unroll
        for (int r = 0; r < 4; ++r) c.CIN[kb][qb][r] = -c.mref[qb];
  }
  asm volatile("s_nop 1" ::: "memory");
}
template <int A> DI void acc_scale(float f) {
  float t; asm volatile("v_accvgpr_read_b32 %0, a%c2\n\tv_mul_f32 %0, %0, %1\n\tv_accvgpr_write_b32 a%c2, %0" : "=&v"(t) : "v"(f), "i"(A) : ALL_AGPRS);
}
template <int A, int OFF, int AUX> DI void ld_acc16(unsigned voff, srd_t srd, unsigned soff) {
  asm volatile("s_nop 4\n\tbuffer_load_dwordx4 a[%c3:%c4], %0, %1, %2 offen offset:%c5 sc1" :: "v"(voff), "s"(srd), "s"(soff), "i"(A), "i"(A + 3), "i"(OFF) : "memory");
}
template <int A, int OFF> DI void ld_acc16n(unsigned voff, srd_t srd, unsigned soff) {
  asm volatile("buffer_load_dwordx4 a[%c3:%c4], %0, %1, %2 offen offset:%c5 sc1" :: "v"(voff), "s"(srd), "s"(soff), "i"(A), "i"(A + 3), "i"(OFF) : "memory");
}
template <int OFF> DI void ld_f32c(float& r, unsigned voff, srd_t srd) { asm volatile("buffer_load_dword %0, %1, %2, 0 offen offset:%c3 sc1" : "=v"(r) : "v"(voff), "s"(srd), "i"(OFF) : "memory"); }
DI void rescale_O(Att& c) {
  asm volatile("s_nop 7" ::: "memory");
  sfor<128>([&](auto I) { constexpr int a = I; acc_scale<a>(c.fct[(a >> 2) & 1]); });
  c.l[0] *= c.fct[0]; c.l[1] *= c.fct[1];
  asm volatile("s_nop 1" ::: "memory");
  c.fct[0] = 1.f; c.fct[1] = 1.f; c.pend = false;
}
template <int G8> DI void dmaK(Att& c, unsigned soff) { dma16c<1024 * (G8 & 3)>(c.vlane, c.srdK, (G8 >> 2) ? soff + 4096u : soff); }
template <int G8> DI void dmaKs(Att& c, unsigned soff) { dma16cs<1024 * (G8 & 3)>(c.vlane, c.srdK, (G8 >> 2) ? soff + 4096u : soff); }
template <int G8> DI void dmaVs(Att& c, unsigned soff) { dma16cs<1024 * (G8 & 3)>(c.vlane, c.srdV, (G8 >> 2) ? soff + 4096u : soff); }
template <int G8> DI void dmaV(Att& c, unsigned soff) { dma16c<1024 * (G8 & 3)>(c.vlane, c.srdV, (G8 >> 2) ? soff + 4096u : soff); }

template <int X, bool FIRST, class Mid> DI void phaseA(Att& c, Mid mid) {
  sfor<64>([&](auto G) {
    constexpr int g = G, k = g >> 1, qb = g & 1, kb = k >> 3, ds = k & 7;
    maybe_wait<g>();
    if constexpr (ds == 0) qk_first<AF_(k), AQ_(qb, 0)>(c.S[X][kb][qb], c.CIN[kb][qb]);
    else qk_next<AF_(k), AQ_(qb, ds)>(c.S[X][kb][qb]);
    if constexpr (g == 50) { waitvl<0>(); BAR(); mid(); }
    if constexpr (!FIRST || g < 50) do_reads<g, X>(c);
    if constexpr (!FIRST) {
      if constexpr ((g & 1) == 1 && g <= 31) do_exp<X ^ 1, 1, (g >> 1)>(c);
      if constexpr ((g & 1) == 0 && g <= 14) do_pack<X ^ 1, 0, (g >> 1)>(c);
      if constexpr ((g & 1) == 0 && g >= 34 && g <= 48) do_pack<X ^ 1, 1, ((g - 34) >> 1)>(c);
    }
    SB();
  });
}
template <int X, bool FIRST> DI void phaseB(Att& c, unsigned m0k, unsigned ksoff, unsigned m0v, unsigned vsoff) {
  sfor<68>([&](auto G) {
    constexpr int g = G;
    if constexpr (!FIRST) {
      maybe_wait<64 + g>();
      if constexpr (g < 2) l_mfma0(c.LT[g & 1], c.ones, c.P[g & 1][0]);
      else if constexpr (g < 4) l_mfma1(c.LT[g & 1], c.ones, c.P[g & 1][1]);
      else { constexpr int v = (g - 4) >> 1, qb = g & 1, db = v >> 1, ks = v & 1; pv_mfma<AO_(db, qb), AF_(32 + v)>(c.P[qb][ks]); }
    }
    if constexpr (FIRST && g == 0) asm volatile("s_nop 7" ::: "memory");
    if constexpr (!FIRST || 64 + g >= rdgap(0) + 132) do_reads<64 + g, X>(c);
    if constexpr (!FIRST) {
      if constexpr (g == 40) { pin(c.LT[0]); c.l[0] += c.LT[0][0]; pin(c.l[0]); }
      if constexpr (g == 42) { pin(c.LT[1]); c.l[1] += c.LT[1][0]; pin(c.l[1]); }
      if constexpr ((g & 1) == 1 && g <= 31) do_max<X, (g >> 1)>(c);
    }
    if constexpr (g == 0) setm0(m0v);
    if constexpr (g == 1 || g == 3 || g == 21 || g == 23) { constexpr int j = g == 1 ? 0 : (g == 3 ? 1 : (g == 21 ? 2 : 3)); if constexpr (FIRST) dmaVs<j>(c, vsoff); else dmaV<j>(c, vsoff); }
    if constexpr (g == 23) setm0(m0v + 4096u);
    if constexpr (g == 25 || g == 27 || g == 29 || g == 31) { if constexpr (FIRST) dmaVs<4 + ((g - 25) >> 1)>(c, vsoff); else dmaV<4 + ((g - 25) >> 1)>(c, vsoff); }
    if constexpr (g == 33) setm0(m0k);
    if constexpr (g == 34 || g == 36 || g == 40 || g == 42) { constexpr int j = g == 34 ? 0 : (g == 36 ? 1 : (g == 40 ? 2 : 3)); if constexpr (FIRST) dmaKs<j>(c, ksoff); else dmaK<j>(c, ksoff); }
    if constexpr (g == 42) setm0(m0k + 4096u);
    if constexpr (g == 44 || g == 46 || g == 48 || g == 50) { if constexpr (FIRST) dmaKs<4 + ((g - 44) >> 1)>(c, ksoff); else dmaK<4 + ((g - 44) >> 1)>(c, ksoff); }
    if constexpr (g == 32) {
      if constexpr (FIRST) raise_max<X, true>(c);
      else { if (__builtin_expect(__any(c.mx2 > THR), 0)) raise_max<X, false>(c); }
    }
    if constexpr ((g & 1) == 1 && g >= 35 && g <= 65) do_exp<X, 0, ((g - 35) >> 1)>(c);
    SB();
  });
}
template <int X, int KIND, class Start, class Pre, class Step, class Fin> DI void drain(Att& c, Start start, Pre pre, Step step, Fin fin) {
  start(IC<KIND>{});
  sfor<16>([&](auto I) { do_exp<X, 1, I>(c); });
  asm volatile("s_nop 0");
  sfor<8>([&](auto I) { do_pack<X, 0, I>(c); });
  sfor<8>([&](auto I) { do_pack<X, 1, I>(c); });
  waitvl<0>();
  BAR();
  pre(IC<KIND>{});
  sfor<12>([&](auto I) { frag_rd<AF_(32 + I), I * 1024>(c.vb[X]); });
  SB();
  sfor<68>([&](auto G) {
    constexpr int g = G;
    if constexpr (g >= 4 && (g & 1) == 0) { constexpr int v = (g - 4) >> 1; waitl<(v + 13 < 32 ? 13 : (31 - v))>(); }
    if constexpr (g < 2) l_mfma0(c.LT[g & 1], c.ones, c.P[g & 1][0]);
    else if constexpr (g < 4) l_mfma1(c.LT[g & 1], c.ones, c.P[g & 1][1]);
    else { constexpr int v = (g - 4) >> 1, qb = g & 1, db = v >> 1, ks = v & 1; pv_mfma<AO_(db, qb), AF_(32 + v)>(c.P[qb][ks]); }
    if constexpr ((g & 1) == 0 && g / 2 + 12 < 32) frag_rd<AF_(32 + g / 2 + 12), (g / 2 + 12) * 1024>(c.vb[X]);
    if constexpr (KIND != 0) step(IC<KIND>{}, G);
    if constexpr (g == 40) { pin(c.LT[0], c.LT[1]); c.l[0] += c.LT[0][0]; c.l[1] += c.LT[1][0]; pin(c.l[0], c.l[1]); }
    SB();
  });
  fin(IC<KIND>{});
}


struct P3 {
  srd_t srdW, srdP;
  unsigned vW[4];
  unsigned bw[3][4];
  unsigned bB[4];
  unsigned vlane, m0s[4], sW, pbase;
  float f[8];
};
constexpr int A3(int par, int nb, int qb) { return 32 * par + 8 * nb + 4 * qb; }
constexpr int R3(int F) { return 208 + 4 * (F % 12); }
constexpr int P3_LA = 16;
template <int A> DI float acc_rd1() { float r; asm volatile("v_accvgpr_read_b32 %0, a%c1" : "=v"(r) : "i"(A)); return r; }
DI void p3_issue_tile(P3& c, unsigned m0v, unsigned soff) {
  setm0(m0v); nop1();
  dma16s<0>(c.vW[0], c.srdW, soff); dma16s<1024>(c.vW[1], c.srdW, soff); dma16s<2048>(c.vW[2], c.srdW, soff); dma16s<3072>(c.vW[3], c.srdW, soff);
  setm0(m0v + 4096u); nop1();
  const unsigned s2 = soff + 4u * 7168u;
  dma16s<0>(c.vW[0], c.srdW, s2); dma16s<1024>(c.vW[1], c.srdW, s2); dma16s<2048>(c.vW[2], c.srdW, s2); dma16s<3072>(c.vW[3], c.srdW, s2);
}
template <int S> DI void p3_tile_step(P3& c, unsigned m0v, unsigned soff) {
  if constexpr (S == 0) setm0(m0v);
  else if constexpr (S <= 4) dma16o<1024 * (S - 1)>(c.vW[S - 1], c.srdW, soff);
  else if constexpr (S == 5) setm0(m0v + 4096u);
  else dma16o<1024 * (S - 6)>(c.vW[S - 6], c.srdW, soff + 4u * 7168u);
}
template <int CG, int F, int I> DI void p3_rd(P3& c) {
  constexpr int nb = F / 9, u = F % 9, reg = R3(F), sl = CG & 3;
  if constexpr (u < 8) tr_rd<reg + 2 * I, (sl == 1 ? 32768 : 0) + 4096 * u + 2048 * I>(c.bw[sl < 2 ? 0 : sl - 1][nb]);
  else tr_rd<reg + 2 * I, 1024 * CG + 16384 * I>(c.bB[nb]);
}
template <int CG>
DI void p3_group(P3& c, const u32x4 (&C)[2][8], const u32x4 (&OB)[2]) {
  constexpr int PAR = CG & 1;
  unsigned k0 = 0, k1 = 0, k2 = 0;
  sfor<72>([&](auto GG) {
    constexpr int g = GG, F = g >> 1, qb = g & 1, nb = F / 9, u = F % 9, reg = R3(F);
    if constexpr (qb == 0 && (F % 2) == 0) waitl<(CG == 6 && g + 16 > 72) ? (72 - g - 4 > 0 ? 72 - g - 4 : 0) : 12>();
    if constexpr (u < 8) mfK<A3(PAR, nb, qb), reg, u == 0>(C[qb][u]); else mfK<A3(PAR, nb, qb), reg, false>(OB[qb]);
    if constexpr (g == 40 && CG < 6) {
      constexpr int N = (CG + 2 <= 6 ? 8 : 0) + (CG >= 1 ? 4 : 0) + (CG >= 2 ? 4 : 0);
      waitv<(CG < 2 ? 63 : N)>(); BAR();
    }
    { constexpr int t = g + P3_LA, Fr = t >> 1, ir = t & 1;
      if constexpr (Fr < 36) p3_rd<CG, Fr, ir>(c); else if constexpr (CG < 6) p3_rd<CG + 1, Fr - 36, ir>(c); }
    if constexpr (CG + 3 <= 6) {
      constexpr int sl = (CG + 3) & 3;
      const unsigned so = c.sW + 128u * (CG + 3);
      if constexpr (g == 42) setm0(c.m0s[sl]);
      if constexpr (g == 43) dma16o<0>(c.vW[0], c.srdW, so);
      if constexpr (g == 45) dma16o<1024>(c.vW[1], c.srdW, so);
      if constexpr (g == 47) dma16o<2048>(c.vW[2], c.srdW, so);
      if constexpr (g == 49) dma16o<3072>(c.vW[3], c.srdW, so);
      if constexpr (g == 50) setm0(c.m0s[sl] + 4096u);
      if constexpr (g == 51) dma16o<0>(c.vW[0], c.srdW, so + 4u * 7168u);
      if constexpr (g == 53) dma16o<1024>(c.vW[1], c.srdW, so + 4u * 7168u);
      if constexpr (g == 55) dma16o<2048>(c.vW[2], c.srdW, so + 4u * 7168u);
      if constexpr (g == 57) dma16o<3072>(c.vW[3], c.srdW, so + 4u * 7168u);
    }
    if constexpr (CG >= 1) {
      sfor<4>([&](auto SS) {
        constexpr int s = SS, q2 = s >> 1, h2 = s & 1, g0 = 2 + 9 * s;
        if constexpr (g >= g0 && g < g0 + 8) { constexpr int e = g - g0; c.f[e] = acc_rd1<A3(PAR ^ 1, 2 * h2 + (e >> 2), q2) + (e & 3)>(); }
        if constexpr (g == g0 + 4) { k0 = cvtpk(c.f[0], c.f[1]); pin(k0); }
        if constexpr (g == g0 + 5) { k1 = cvtpk(c.f[2], c.f[3]); pin(k1); }
        if constexpr (g == g0 + 7) { k2 = cvtpk(c.f[4], c.f[5]); pin(k2); }
        if constexpr (g == g0 + 8) { const u32x4 pk = {k0, k1, k2, cvtpk(c.f[6], c.f[7])};
          st16<16>(c.srdP, c.vlane, c.pbase + (unsigned)((CG - 1) * 2048 + q2 * 14 * 1024 + h2 * 1024), pk); }
      });
    }
    SB();
  });
}

constexpr size_t FL_GEN = 0, FL_START = 4096, FL_KV = 8192, FL_Q = 16384, FL_O = 49152, FL_P = 81920;
typedef __attribute__((address_space(1))) unsigned gu32;
DI void flag_set(char* base, unsigned byteoff, unsigned tag) { __hip_atomic_store((gu32*)(base + byteoff), tag, __ATOMIC_RELAXED, __HIP_MEMORY_SCOPE_AGENT); }
DI unsigned flag_get(char* base, unsigned byteoff) { return __hip_atomic_load((gu32*)(base + byteoff), __ATOMIC_RELAXED, __HIP_MEMORY_SCOPE_AGENT); }
DI void ld_flag(unsigned& r, const char* p) { asm volatile("s_nop 4\n\tglobal_load_dword %0, %1, off sc1" : "=v"(r) : "v"(p) : "memory"); }
DI void poll_all(char* base, unsigned byteoff, unsigned tag) {
  unsigned spins = 0;
  while (!__all(flag_get(base, byteoff) == tag)) { if (++spins > (1u << 16)) break; __builtin_amdgcn_s_sleep(1); }
}

__global__ __launch_bounds__(256, 1) void fused_main(const bf16_t* __restrict__ x, const bf16_t* __restrict__ ckv, const bf16_t* __restrict__ wq, const bf16_t* __restrict__ wuk,
                                                    const bf16_t* __restrict__ wuv, const bf16_t* __restrict__ wo, const bf16_t* __restrict__ wqkv, const bf16_t* __restrict__ woB,
                                                    const bf16_t* __restrict__ gw, char* __restrict__ ws, bf16_t* __restrict__ out) {
  extern __shared__ __attribute__((aligned(16))) char smem[];
  LAS char* lds = (LAS char*)smem;
  const unsigned ldsb = (unsigned)(size_t)lds;
  asm volatile("" ::: ALL_AGPRS);
  unsigned long long code0; unsigned codelen;
  asm volatile("s_getpc_b64 %0\nfused_code_begin:\n\ts_mov_b32 %1, fused_code_end-fused_code_begin" : "=s"(code0), "=s"(codelen));
  const int b = blockIdx.x, xcd = b & 7, i = b >> 3;
  const int head = 2 * xcd + (i & 1);
  const unsigned lane = threadIdx.x & 63u, w = rfl(threadIdx.x >> 6);
  char* fl = ws + WS_FLAG;
  const srd_t srdH = mksrd(ws + (size_t)head * (1u << 20), 100u << 20);
  constexpr unsigned HQ = (unsigned)WS_Q, HPO = (unsigned)WS_PO;
  const unsigned hPART = (unsigned)WS_PART - (unsigned)head * (1u << 20);
  const unsigned hLSE = (unsigned)WS_LSE - (unsigned)head * (1u << 20);
  const unsigned gen = *(const __attribute__((address_space(4))) unsigned*)(fl + FL_GEN);
  const int isV = (i >> 1) & 1, tt = i >> 2;
  const unsigned tag = gen * 1664525u + 1013904223u;
  u32x4 kvp[32];
  P1 p1;
  const int pi = (i >> 1) & 7, role = i >> 4;
  constexpr int NA = 18;
  const int tl = 15 - pi, nl = 2 * (tl + 1);
  const int town = role ? pi : tl;
  {
    const unsigned wr = w >> 1, wc = w & 1u;
    XStream X; WStream W;
    X.srd = mksrd(ckv, SEQ * DC * 2);
#pragma unroll
    for (int j = 0; j < 8; ++j) X.v[j] = (8u * j + (lane >> 3)) * 2048u + 16u * ((lane & 7u) ^ (4u * (j & 1) + (lane >> 4))) - 1024u * (j & 3);
    X.soff = (256u * tt + 64u * w) * 2048u;
    makeW4(W, isV ? wuv : wuk, DC * 4096 * 2, 8192, head * 256, w, lane);
    unsigned xb[2], wb[8];
    {
      const unsigned r = lane & 15u, g = lane >> 4, q = (lane >> 2) & 3u, p = lane & 3u;
#pragma unroll
      for (int e = 0; e < 2; ++e) xb[e] = 128u * (128u * wr + r) + 16u * ((4u * e + g) ^ ((r >> 1) & 7u));
#pragma unroll
      for (int n = 0; n < 8; ++n) wb[n] = WBASE + 512u * (8u * g + q) + 32u * ((8u * wc + n) ^ ((q << 1) | (g & 1u))) + 8u * p;
    }
    issueX(X, ldsb + w * 8192u, 0);
    issueW(W, ldsb + WBASE + 0 * WSLOT + w * 4096u); issueX(X, ldsb + w * 8192u, 1);
    issueW(W, ldsb + WBASE + 1 * WSLOT + w * 4096u); issueX(X, ldsb + XSLOT + w * 8192u, 0);
    issueW(W, ldsb + WBASE + 2 * WSLOT + w * 4096u); issueX(X, ldsb + XSLOT + w * 8192u, 1);
    issueW(W, ldsb + WBASE + 3 * WSLOT + w * 4096u); issueX(X, ldsb + 2 * XSLOT + w * 8192u, 0);
    unsigned pj[1];
    { const srd_t scode = mksrd((const void*)code0, codelen);
      pf_load(pj[0], (w == 3u) ? ((unsigned)i * 64u + lane) * 128u : 0x7fffff00u, scode); }
    Prefetch pf;
    {
      const unsigned ci = (unsigned)i >> 1;
      pf.j0 = 0u; pf.so = 0u;
      if (w == 0) { pf.s0 = mksrd(wq, DM * 4096 * 2); pf.v0 = lane < 8u ? (28u * ci + (lane >> 2)) * 8192u + head * 512u + (lane & 3u) * 128u : 0x7fffff00u; pf.step = 16384u; }
      else if (w == 1) { pf.s0 = mksrd(wo, 4096 * DM * 2); pf.v0 = lane < 8u ? (unsigned)(head * 256 + 16 * (int)ci) * 896u + lane * 128u : 0x7fffff00u; pf.step = 1024u; }
      else if (w == 2) { pf.s0 = mksrd(wqkv, DM * 1344 * 2); pf.v0 = lane < 3u ? ((unsigned)b * 37u + lane) * 128u : 0x7fffff00u; pf.step = 384u; }
      else { pf.s0 = mksrd(x, SEQ * DM * 2); pf.v0 = lane < 4u ? ((unsigned)b * 56u + lane) * 128u : 0x7fffff00u; pf.step = 512u; }
    }
    { const u32x4 z = zero4(); nop1(); nop1(); sfor<16>([&](auto I) { clear16<16 * I>(z); }); }
    waitv<25>(); BAR();
    Frags f0, f1;
#pragma unroll
    for (int n = 0; n < 8; ++n) { f0.wl[n] = readWh(lds, wb[n], 0); f0.wh[n] = readWh(lds, wb[n], 2048); }
#pragma unroll
    for (int m = 0; m < 8; ++m) f0.x[m] = readX(lds, xb[0], 2048 * m);
    f0.join();
    p1_setup(p1, x, wq, wqkv, head, town, w, lane);
    const srd_t sg = mksrd(gw, DM * 2);
    auto hook = [&](auto E4, auto I) { p1_preamble<decltype(E4)::value, decltype(I)::value>(p1, sg, ldsb, w, lane); };
    if (isV) kv_loop<true>(f0, f1, lds, ldsb, X, W, xb, wb, w, pf, hook);
    else kv_loop<false>(f0, f1, lds, ldsb, X, W, xb, wb, w, pf, hook);
    pin(pf.j0); pin(pj[0]);
    snop<7>();
    {
      const unsigned wr2 = w >> 1, wc2 = w & 1u, vl = lane * 16u;
      const unsigned base = (isV ? (unsigned)WS_V : 0u) + (4u * tt + 2u * wr2) * 32768u + wc2 * (isV ? 16384u : 4096u);
      if (isV) sfor<32>([&](auto I) { constexpr int mp = I / 8, n = I % 8;
        if constexpr (I < KV_PRE) st16<16>(srdH, vl, base + (unsigned)((mp >> 1) * 32768 + n * 2048 + (mp & 1) * 1024), acc_pack1<4 * (8 * (2 * mp) + n), 4 * (8 * (2 * mp + 1) + n)>());
        else if constexpr (I == KV_PRE) kvp[I] = acc_pack1<4 * (8 * (2 * mp) + n), 4 * (8 * (2 * mp + 1) + n)>();
        else kvp[I] = acc_pack1n<4 * (8 * (2 * mp) + n), 4 * (8 * (2 * mp + 1) + n)>();
        SB(); });
      else sfor<32>([&](auto I) { constexpr int m = I / 4, np = I % 4;
        if constexpr (I < KV_PRE) st16<16>(srdH, vl, base + (unsigned)((m >> 2) * 32768 + (m & 3) * 8192 + np * 1024), acc_pack1<4 * (8 * m + 2 * np), 4 * (8 * m + 2 * np + 1)>());
        else if constexpr (I == KV_PRE) kvp[I] = acc_pack1<4 * (8 * m + 2 * np), 4 * (8 * m + 2 * np + 1)>();
        else kvp[I] = acc_pack1n<4 * (8 * m + 2 * np), 4 * (8 * m + 2 * np + 1)>();
        SB(); });
    }
    if (threadIdx.x == 0) flag_set(fl, FL_START + b * 4u, tag);
  }
  P1Out po;
  unsigned kvflag = 0u;
  Att c;
  c.vlane = lane * 16u;
  {
    const unsigned wr = w >> 1, wc = w & 1u;
    const srd_t so = srdH;
    const unsigned base = (isV ? (unsigned)WS_V : 0u) + (4u * tt + 2u * wr) * 32768u + wc * (isV ? 16384u : 4096u);
    auto st = [&](auto I) {
      constexpr int ii = I;
      const unsigned off = isV ? (unsigned)(((ii / 8) >> 1) * 32768 + (ii % 8) * 2048 + ((ii / 8) & 1) * 1024) : (unsigned)(((ii / 4) >> 2) * 32768 + ((ii / 4) & 3) * 8192 + (ii % 4) * 1024);
      st16<16>(so, c.vlane, base + off, kvp[ii]);
    };
    auto done = [&] { if (lane == 0) flag_set(fl, FL_KV + (unsigned)(head * 64 + (i >> 1) * 4 + (int)w) * 4u, tag); };
    auto early = [&] { ld_flag(kvflag, fl + FL_KV + (unsigned)(head * 64) * 4u + lane * 4u); };
    p1_loop<1>(p1, ldsb, w, lane, st, done, early);
  }
  const bool split = nl > NA;
  const int nseg = role ? 2 : 1, seg0 = (role && !split) ? 1 : 0;
  const int k01 = role ? NA : 0, n1 = role ? (split ? nl - NA : 0) : (split ? NA : nl), n2 = role ? 2 * (pi + 1) : 0;
  c.srdK = mksrd(ws + WS_K + (size_t)head * (1u << 20), 1u << 20);
  c.srdV = mksrd(ws + WS_V + (size_t)head * (1u << 20), 1u << 20);
  { u32x4 o = {0x3f803f80u, 0x3f803f80u, 0x3f803f80u, 0x3f803f80u}; asm volatile("" : "+v"(o)); c.ones = o; }
  auto toff = [&](int jj) -> unsigned {
    unsigned t = (jj < n1) ? (unsigned)(k01 + jj) : (unsigned)(jj - n1);
    unsigned o = t * 32768u + w * 8192u;
    return (jj < n1 + n2) ? o : 0x40000000u;
  };
  const unsigned mK = ldsb + w * 8192u, mV = ldsb + 65536u + w * 8192u;
  auto dmaK8 = [&](int jj) { const unsigned so = toff(jj), m0 = mK + (jj & 1) * 32768u;
    setm0(m0); nop1(); sfor<4>([&](auto I) { dmaKs<I>(c, so); }); setm0(m0 + 4096u); nop1(); sfor<4>([&](auto I) { dmaKs<4 + I>(c, so); }); };
  auto loadQ = [&](int qt) {
    const unsigned so = HQ + (unsigned)(8 * qt + 2 * w) * 8192u;
    sfor<16>([&](auto I) { constexpr int qb = I >> 3, ds = I & 7; ld_acc16<AQ_(qb, ds), (I & 3) * 1024, 16>(c.vlane, srdH, so + (unsigned)(I >> 2) * 4096u); });
  };
  const unsigned fQ = FL_Q + (unsigned)((head * 8 + pi) * 4 + (int)w) * 64u, fO = FL_O + (unsigned)((head * 8 + pi) * 4 + (int)w) * 64u;
  auto poll1 = [&](unsigned off) { unsigned spins = 0; while (rfl(flag_get(fl, off)) != tag) { if (++spins > (1u << 16)) break; __builtin_amdgcn_s_sleep(1); } };
  waitv<0>(); pin(kvflag);
  if (!__all(kvflag == tag)) poll_all(fl, FL_KV + (unsigned)(head * 64) * 4u + lane * 4u, tag);
  BAR();
  dmaK8(0); dmaK8(1);
  P1B pb;
  snop<7>(); snop<3>();
  p1_post_b1(pb, po, lane);
  {
    const unsigned so = HQ + (unsigned)(8 * town + 2 * (int)w) * 8192u;
    p1_post_q([&](auto I, const u32x4& v) { acc_wr4<AQ_(I >> 3, I & 7)>(v); st16<16>(srdH, c.vlane + (I & 3) * 1024u, so + (unsigned)(I >> 2) * 4096u, v); });
  }
  snop<1>();
  { const u32x4 z = zero4(); nop1(); nop1(); sfor<8>([&](auto I) { clear16<16 * I>(z); }); }
  p1_post_b2(pb, po, town, w, lane);
  waitv<0>();
  if (!role && split && lane == 0) flag_set(fl, fQ, tag);
  if (role && split) { poll1(fQ); loadQ(tl); }
  P3 p3;
  int jb = 0;
  bool pendO = false;
  for (int seg = seg0; seg < nseg; ++seg) {
    const int qt = seg ? town : tl, k0 = seg ? 0 : k01, n = seg ? n2 : n1;
    const bool lastseg = seg + 1 == nseg;
    const int q0 = 128 * qt + 32 * w;
    const unsigned par = jb & 1;
    c.kb[0] = ldsb + c.vlane + par * 32768u; c.kb[1] = ldsb + c.vlane + (par ^ 1) * 32768u;
    c.vb[0] = ldsb + c.vlane + 65536u + par * 32768u; c.vb[1] = ldsb + c.vlane + 65536u + (par ^ 1) * 32768u;
    c.l[0] = c.l[1] = 0.f;
    c.mref[0] = c.mref[1] = 0.f; c.fct[0] = c.fct[1] = 1.f; c.pend = false;
    const int dqb = q0 + (int)(lane & 15u) - 4 * (int)(lane >> 4);
    auto isdiag = [&](int t) { return (k0 + t) >= 2 * qt; };
    build_cin(c, isdiag(0), dqb - 64 * k0);
    if (seg == seg0) { waitv<0>(); BAR(); }
    sfor<NPRE>([&](auto I) { frag_rd<AF_(I), I * 1024>(c.kb[0]); });
    waitl<0>();
    SB();
    const unsigned so = HPO + (unsigned)(8 * qt + 2 * w) * 8192u;
    float* lsep = (float*)(ws + WS_LSE) + head * 2048 + q0 + (lane & 15u);
    float lseB[2] = {0.f, 0.f};
    unsigned oflag = 0u, vbB = 0u, sW = 0u, sWB = 0u, m0B = 0u, lse_a = 0u;
    srd_t sB = p3.srdW;
    auto dstart = [&](auto K) { if constexpr (K == 2) ld_flag(oflag, fl + fO); };
    auto dpre = [&](auto K) {
      if constexpr (K != 0) {
        if constexpr (K == 2) { pin(oflag); if (rfl(oflag) != tag) poll1(fO); }
        const unsigned g = lane >> 4, q = (lane >> 2) & 3u, p = lane & 3u;
        p3.srdW = mksrd(wo, 4096 * DM * 2);
#pragma unroll
        for (int jj = 0; jj < 4; ++jj) p3.vW[jj] = (8u * jj + (lane >> 3)) * 896u + 16u * ((lane & 7u) ^ (((lane >> 4) & 3u) << 1)) - 1024u * jj;
        sW = (unsigned)(head * 256 + 64 * (int)w) * 896u;
        sB = mksrd((const char*)woB - 4096, DM * DM * 2 + 4096);
        vbB = 4096u + (lane >> 3) * 896u + 16u * ((lane & 7u) ^ (((lane >> 4) & 3u) << 1));
        m0B = mV + ((jb + n) & 1) * 32768u; sWB = (unsigned)(28 * head + 8 * (int)w) * 896u;
        const unsigned r = 4u * g + q, swz = (r >> 1) & 3u;
        const unsigned vlast = 65536u + ((jb + n + 1) & 1) * 32768u;
#pragma unroll
        for (int nb = 0; nb < 4; ++nb) {
          const unsigned b0 = ldsb + 128u * r + 32u * ((unsigned)nb ^ swz) + 8u * p;
          p3.bw[0][nb] = b0; p3.bw[1][nb] = b0 + 131072u; p3.bw[2][nb] = b0 + vlast;
          p3.bB[nb] = ldsb + 65536u + ((jb + n) & 1) * 32768u + (r >> 3) * 8192u + (r & 7u) * 128u + 32u * ((unsigned)nb ^ swz) + 8u * p;
        }
        p3.m0s[0] = mK; p3.m0s[1] = mK + 32768u; p3.m0s[2] = ldsb + 131072u + w * 8192u; p3.m0s[3] = ldsb + vlast + w * 8192u;
        p3.sW = sW;
        lse_a = hLSE + (unsigned)(head * 2048 + q0 + (int)(lane & 15u)) * 4u;
      }
    };
    auto dstep = [&](auto K, auto G) {
      constexpr int k0_ = (int)G - 1 - (K == 2 ? 18 : 0);
      if constexpr (K == 2 && G >= 1 && G <= 18) {
        constexpr int j = (int)G - 1;
        if constexpr (j < 2) ld_f32c<64 * j>(lseB[j], lse_a, srdH);
        else { constexpr int I = j - 2, qb = I >> 3, u = I & 7; ld_acc16n<AQ_(qb, u), (u & 3) * 1024>(c.vlane, srdH, so + (unsigned)(2 * qb + (u >> 2)) * 4096u); }
      } else if constexpr (k0_ >= 0 && k0_ < 39) {
        if constexpr (k0_ < 10) p3_tile_step<k0_>(p3, p3.m0s[0], sW);
        else if constexpr (k0_ < 19) {
          constexpr int s = k0_ - 10;
          if constexpr (s == 0) setm0(m0B);
          else if constexpr (s <= 4) dma16o<1024 * (s - 1)>(vbB + (unsigned)(128 * (s - 1) - 1024 * (s - 1)), sB, sWB);
          else if constexpr (s == 5) setm0(m0B + 4096u);
          else dma16o<1024 * (s - 6)>(vbB + (unsigned)(128 * (s - 2) - 1024 * (s - 6)), sB, sWB);
        }
        else if constexpr (k0_ < 29) p3_tile_step<k0_ - 19>(p3, p3.m0s[1], sW + 128u);
        else p3_tile_step<k0_ - 29>(p3, p3.m0s[2], sW + 256u);
      }
    };
    auto dfin = [&](auto K) { if constexpr (K == 2) { waitv<31>(); pin(lseB[0], lseB[1]); } };
    auto run_drain = [&](auto XX) {
      constexpr int X_ = XX;
      if (!lastseg) drain<X_, 0>(c, dstart, dpre, dstep, dfin);
      else if (!role && split) drain<X_, 2>(c, dstart, dpre, dstep, dfin);
      else drain<X_, 1>(c, dstart, dpre, dstep, dfin);
    };
    auto nomid = [] {};
    phaseA<0, true>(c, [&] { if (pendO) { if (lane == 0) flag_set(fl, fO, tag); pendO = false; } });
    phaseB<0, true>(c, mK + ((jb + 2) & 1) * 32768u, toff(jb + 2), mV + (jb & 1) * 32768u, toff(jb));
    if (1 < n) { build_cin(c, isdiag(1), dqb - 64 * (k0 + 1)); }
    int t = 1;
    for (;;) {
      if (t >= n) { run_drain(IC<0>{}); break; }
      phaseA<1, false>(c, nomid);
      if (t == n - 1 && !lastseg) loadQ(town);
      phaseB<1, false>(c, mK + ((jb + t) & 1) * 32768u, toff(jb + t + 2), mV + ((jb + t) & 1) * 32768u, toff(jb + t));
      { const bool nd = (t + 1 < n) && isdiag(t + 1);
        if (__builtin_expect(c.pend || nd, 0)) { if (c.pend) rescale_O(c); build_cin(c, nd, dqb - 64 * (k0 + t + 1)); } }
      ++t;
      if (t >= n) { run_drain(IC<1>{}); break; }
      phaseA<0, false>(c, nomid);
      if (t == n - 1 && !lastseg) loadQ(town);
      phaseB<0, false>(c, mK + ((jb + t) & 1) * 32768u, toff(jb + t + 2), mV + ((jb + t) & 1) * 32768u, toff(jb + t));
      { const bool nd = (t + 1 < n) && isdiag(t + 1);
        if (__builtin_expect(c.pend || nd, 0)) { if (c.pend) rescale_O(c); build_cin(c, nd, dqb - 64 * (k0 + t + 1)); } }
      ++t;
    }
    if (n == 1 && !lastseg) loadQ(town);
    asm volatile("s_nop 7" ::: "memory");
    if (!lastseg) {
      float cA[2];
#pragma unroll
      for (int qb = 0; qb < 2; ++qb) {
        cA[qb] = c.l[qb] > 0.f ? __builtin_amdgcn_rcpf(c.l[qb]) : 0.f;
        const float lse = c.l[qb] > 0.f ? c.mref[qb] + __builtin_amdgcn_logf(c.l[qb]) : -1e30f;
        if (lane < 16u) __hip_atomic_store(lsep + 16 * qb, lse, __ATOMIC_RELAXED, __HIP_MEMORY_SCOPE_AGENT);
      }
      sfor<16>([&](auto I) {
        constexpr int qb = I >> 3, u = I & 7;
        st16<16>(srdH, c.vlane + (u & 3) * 1024u, so + (unsigned)(2 * qb + (u >> 2)) * 4096u, acc_pack<AO_(2 * u, qb), AO_(2 * u + 1, qb)>(cA[qb]));
        SB();
      });
      pendO = true;
      snop<1>();
      { const u32x4 z = zero4(); nop1(); nop1(); sfor<8>([&](auto I) { clear16<16 * I>(z); }); }
    } else {
      float cA[2], cB[2] = {0.f, 0.f};
      if (!role && split) {
#pragma unroll
        for (int qb = 0; qb < 2; ++qb) {
          const float lseA = c.mref[qb] + __builtin_amdgcn_logf(c.l[qb]);
          const float M = fmaxf(lseA, lseB[qb]);
          const float wA = __builtin_amdgcn_exp2f(lseA - M), wB = __builtin_amdgcn_exp2f(lseB[qb] - M);
          const float it = po.g[qb] * __builtin_amdgcn_rcpf(wA + wB);
          cA[qb] = wA * it * __builtin_amdgcn_rcpf(c.l[qb]); cB[qb] = wB * it;
        }
      } else {
#pragma unroll
        for (int qb = 0; qb < 2; ++qb) cA[qb] = po.g[qb] * __builtin_amdgcn_rcpf(c.l[qb]);
      }
      u32x4 C[2][8];
      if (!role && split) {
        sfor<16>([&](auto I) {
          constexpr int qb = I >> 3, u = I & 7;
          float f0[4], f1[4], pb[4];
          acc_rd4n<AO_(2 * u, qb)>(f0); acc_rd4n<AO_(2 * u + 1, qb)>(f1); acc_rd4n<AQ_(qb, u)>(pb);
          float o[8];
#pragma unroll
          for (int r = 0; r < 4; ++r) { o[r] = f0[r] * cA[qb]; o[4 + r] = f1[r] * cA[qb]; }
#pragma unroll
          for (int d = 0; d < 4; ++d) {
            o[2 * d] += cB[qb] * __uint_as_float(__float_as_uint(pb[d]) << 16);
            o[2 * d + 1] += cB[qb] * __uint_as_float(__float_as_uint(pb[d]) & 0xffff0000u);
          }
          u32x4 v; v[0] = cvtpk(o[0], o[1]); v[1] = cvtpk(o[2], o[3]); v[2] = cvtpk(o[4], o[5]); v[3] = cvtpk(o[6], o[7]);
          C[qb][u] = v;
          SB();
        });
      } else {
        sfor<16>([&](auto I) { constexpr int qb = I >> 3, u = I & 7; C[qb][u] = acc_packn<AO_(2 * u, qb), AO_(2 * u + 1, qb)>(cA[qb]); SB(); });
      }
      waitv<0>(); BAR();
      p3.srdP = srdH;
      p3.vlane = c.vlane;
      p3.pbase = hPART + (unsigned)head * (128u * 14u * 1024u) + (unsigned)(8 * qt + 2 * (int)w) * (14u * 1024u);
      sfor<16>([&](auto T) { p3_rd<0, (T >> 1), (T & 1)>(p3); });
      waitl<0>();
      sfor<16>([&](auto I) { pin(C[I >> 3][I & 7]); }); pin(po.ob[0], po.ob[1]); snop<1>();
      SB();
      sfor<7>([&](auto CG) { p3_group<CG>(p3, C, po.ob); });
      asm volatile("s_nop 7" ::: "memory");
      sfor<4>([&](auto SS) {
        constexpr int q2 = SS >> 1, h2 = SS & 1;
        st16<16>(p3.srdP, p3.vlane, p3.pbase + 6u * 2048u + (unsigned)(q2 * 14 * 1024 + h2 * 1024), acc_pack1<A3(0, 2 * h2, q2), A3(0, 2 * h2 + 1, q2)>());
      });
    }
    jb += n;
  }
  waitv<0>();
  if (lane == 0) flag_set(fl, FL_P + (unsigned)(town * 64 + head * 4 + (int)w) * 4u, tag);
  {
    const srd_t sp = srdH;
    u32x4 v[2][16];
    unsigned selLo = 0x00003f80u, selHi = 0x3f800000u; pin(selLo, selHi);
    const unsigned un0 = 7u * head + w, un1 = un0 + 4u;
    const unsigned o0 = hPART + ((8u * town + un0 / 14u) * 14u + un0 % 14u) * 1024u, o1 = (w < 3u) ? hPART + ((8u * town + un1 / 14u) * 14u + un1 % 14u) * 1024u : 0x40000000u;
    unsigned long long ready = __ballot(flag_get(fl, FL_P + (unsigned)(town * 64) * 4u + lane * 4u) == tag);
    unsigned spins = 0;
    float a[2][8] = {{0.f, 0.f, 0.f, 0.f, 0.f, 0.f, 0.f, 0.f}, {0.f, 0.f, 0.f, 0.f, 0.f, 0.f, 0.f, 0.f}};
    auto add = [&](int h) {
#pragma unroll
      for (int k = 0; k < 2; ++k)
#pragma unroll
        for (int d = 0; d < 4; ++d) { a[k][2 * d] = dot2bf(v[k][h][d], selLo, a[k][2 * d]); a[k][2 * d + 1] = dot2bf(v[k][h][d], selHi, a[k][2 * d + 1]); }
    };
#pragma unroll
    for (int h = 0; h < 16; ++h) {
      while (((ready >> (4 * h)) & 15ull) != 15ull) {
        if (++spins > (1u << 16)) break;
        __builtin_amdgcn_s_sleep(1);
        ready = __ballot(flag_get(fl, FL_P + (unsigned)(town * 64) * 4u + lane * 4u) == tag);
      }
      v[0][h] = __builtin_amdgcn_raw_buffer_load_b128(sp, lane * 16u, (unsigned)h * (128u * 14u * 1024u) + o0, 16);
      v[1][h] = __builtin_amdgcn_raw_buffer_load_b128(sp, lane * 16u, (unsigned)h * (128u * 14u * 1024u) + o1, 16);
      if (h >= 6) { SB(); add(h - 6); SB(); }
    }
#pragma unroll
    for (int h = 10; h < 16; ++h) add(h);
#pragma unroll
    for (int k = 0; k < 2; ++k) {
      if (k == 1 && w == 3) break;
      const unsigned un = k ? un1 : un0, tb = 8u * town + un / 14u, u = un % 14u;
      bf16_t* o = out + (size_t)(16u * tb + (lane & 15u)) * DM + 32u * u + 4u * (lane >> 4);
      *(uint2*)o = make_uint2(cvtpk(a[k][0], a[k][1]), cvtpk(a[k][2], a[k][3]));
      *(uint2*)(o + 16) = make_uint2(cvtpk(a[k][4], a[k][5]), cvtpk(a[k][6], a[k][7]));
    }
  }
  if (b == 0 && w == 0) {
    for (int r = 0; r < 4; ++r) poll_all(fl, FL_START + (unsigned)(r * 64) * 4u + lane * 4u, tag);
    if (lane == 0) flag_set(fl, FL_GEN, tag);
  }
  asm volatile("fused_code_end:");
}

extern "C" void kernel_launch(void* const* d_in, const int* in_sizes, int n_in, void* d_out, int out_size, void* d_ws, size_t ws_size, hipStream_t stream) {
  (void)hipFuncSetAttribute((const void*)fused_main, hipFuncAttributeMaxDynamicSharedMemorySize, LDS_BYTES);
  fused_main<<<256, 256, LDS_BYTES, stream>>>((const bf16_t*)d_in[0], (const bf16_t*)d_in[1], (const bf16_t*)d_in[2], (const bf16_t*)d_in[3], (const bf16_t*)d_in[4],
                                             (const bf16_t*)d_in[5], (const bf16_t*)d_in[7], (const bf16_t*)d_in[8], (const bf16_t*)d_in[9], (char*)d_ws, (bf16_t*)d_out);
}
```
